```python
import torch

def _softmax(x, axis=-1):
    return torch.softmax(x, dim=axis)

def _gelu(x):
    return torch.nn.functional.gelu(x, approximate='tanh')

def _silu(x):
    return torch.nn.functional.silu(x)

def _sigmoid(x):
    return torch.sigmoid(x)

def _layernorm(x, axis=-1, eps=1e-05):
    m = x.mean(dim=axis, keepdim=True)
    v = x.var(dim=axis, keepdim=True, unbiased=False)
    return (x - m) / torch.sqrt(v + eps)

def _scatter_add(tgt, idx, src, axis=0):
    return tgt.clone().index_add_(axis, idx, src)

def _segment_sum(src, seg, n):
    out = torch.zeros((n,) + src.shape[1:], dtype=src.dtype)
    return out.index_add_(0, seg, src)

def _pad(x, pw):
    flat = tuple((v for pair in reversed(pw) for v in pair))
    return torch.nn.functional.pad(x, flat)
kernel_name = 'adaLN_cond__spectral_gfnet__glu_gated_reglu__prompt'

def setup_inputs():

    _w = lambda a: (lambda t: t.to(torch.bfloat16) if t.is_floating_point() else t)(torch.as_tensor(a))

    def _A_setup():
        B, S, D = (1, 2048, 640)
        Df = D // 2 + 1
        return {'x': _w((lambda *s: torch.randn(*s, dtype=torch.bfloat16) * 0.05)(B, S, D)), 'w_r': _w((lambda *s: torch.randn(*s, dtype=torch.bfloat16) * 1.6)(S, Df) * 0.02), 'w_i': _w((lambda *s: torch.randn(*s, dtype=torch.bfloat16) * 1.6)(S, Df) * 0.02)}
    _A_in = _A_setup()

    def _B_setup():
        B, S, D, F = (1, 2048, 640, 1536)
        return {'x': _w((lambda *s: torch.randn(*s, dtype=torch.bfloat16) * 0.05)(B, S, D)), 'W_gate': _w((lambda *s: torch.randn(*s, dtype=torch.bfloat16) * 0.4)(D, F) * (2.0 / D) ** 0.5), 'W_up': _w((lambda *s: torch.randn(*s, dtype=torch.bfloat16) * 0.4)(D, F) * (2.0 / D) ** 0.5), 'W_down': _w((lambda *s: torch.randn(*s, dtype=torch.bfloat16) * 0.4)(F, D) * (1.0 / F) ** 0.5)}
    _B_in = _B_setup()
    return {'A_x': _A_in['x'], 'A_w_r': _A_in['w_r'], 'A_w_i': _A_in['w_i'], 'B_x': _B_in['x'], 'B_W_gate': _B_in['W_gate'], 'B_W_up': _B_in['W_up'], 'B_W_down': _B_in['W_down'], 'c': _w((lambda *s: torch.randn(*s, dtype=torch.bfloat16) * 0.2)(1, 640) * 8.0), 'w_mod': _w((lambda *s: torch.randn(*s, dtype=torch.bfloat16) * 0.2)(640, 6 * 640) * 640 ** (-0.5) * 8.0)}

def reference(A_x, A_w_r, A_w_i, B_x, B_W_gate, B_W_up, B_W_down, c, w_mod):

    def _A_ref(x, w_r, w_i):
        B, S, D = x.shape
        X = (lambda x, axes=(-2, -1): torch.fft.rfft2(x.float(), dim=axes))(x, axes=(-2, -1))
        W = w_r + 1j * w_i
        X = X * W
        y = (lambda x, s, axes=(-2, -1): torch.fft.irfft2(x, s=s, dim=axes).to(torch.bfloat16))(X, (S, D), axes=(-2, -1))
        return y

    def _B_ref(x, W_gate, W_up, W_down):
        gate = torch.einsum('bsd,df->bsf', x, W_gate)
        up = torch.einsum('bsd,df->bsf', x, W_up)
        gate = gate * (lambda x, dt: x.to(dt))(gate > 0, gate.dtype)
        h = gate * up
        return torch.einsum('bsf,fd->bsd', h, W_down)
    x = A_x
    mod = (_silu(c) @ w_mod).reshape(c.shape[0], 6, 1, -1)
    sa, ca, ga, sb, cb, gb = (mod[:, 0].clone(), mod[:, 1].clone(), mod[:, 2].clone(), mod[:, 3].clone(), mod[:, 4].clone(), mod[:, 5].clone())
    h = _layernorm(x, axis=-1) * (1 + ca) + sa
    x = x + ga * _A_ref(h, A_w_r, A_w_i)
    h = _layernorm(x, axis=-1) * (1 + cb) + sb
    x = x + gb * _B_ref(h, B_W_gate, B_W_up, B_W_down)
    return x
```

```cpp
#include <hip/hip_runtime.h>
#include <hip/hip_bf16.h>
#include <stdint.h>
typedef unsigned short u16;
#define S 2048
#define D 640
#define DF 321
#define F 1536
__device__ __forceinline__ float bf2f(u16 v){ return __uint_as_float(((unsigned)v)<<16); }
__device__ __forceinline__ u16 f2bf(float f){ unsigned u=__float_as_uint(f); u += 0x7fffu + ((u>>16)&1u); return (u16)(u>>16); }
__device__ __forceinline__ float rbf(float f){ return bf2f(f2bf(f)); }

__global__ void k_mod(const u16* c, const u16* wm, float* mod){
  int j = blockIdx.x*blockDim.x+threadIdx.x; if(j>=6*D) return;
  float acc=0; for(int k=0;k<D;k++){ float cv=bf2f(c[k]); float s=rbf(cv/(1.f+__expf(-cv))); acc += s*bf2f(wm[k*6*D+j]); }
  mod[j]=rbf(acc);
}
__global__ void k_ln(const float* x, const float* shift, const float* scale, float* h){
  int s=blockIdx.x; __shared__ float red[256];
  int t=threadIdx.x; float a=0; for(int d=t;d<D;d+=256) a+=x[s*D+d];
  red[t]=a; __syncthreads(); for(int o=128;o>0;o>>=1){ if(t<o) red[t]+=red[t+o]; __syncthreads(); }
  float mf=red[0]/D; __syncthreads();
  a=0; for(int d=t;d<D;d+=256){ float q=x[s*D+d]-mf; a+=q*q; }
  red[t]=a; __syncthreads(); for(int o=128;o>0;o>>=1){ if(t<o) red[t]+=red[t+o]; __syncthreads(); }
  float v=rbf(red[0]/D); float m=rbf(mf);
  float den=rbf(sqrtf(rbf(v+1e-5f)));
  for(int d=t;d<D;d+=256){ float q=rbf(rbf(x[s*D+d]-m)/den); h[s*D+d]=rbf(rbf(q*rbf(1.f+scale[d]))+shift[d]); }
}
__global__ void k_cvt(const u16* x, float* o){ int i=blockIdx.x*256+threadIdx.x; o[i]=bf2f(x[i]); }
__global__ void k_rowdft(const float* h, float2* R){
  int s=blockIdx.x; __shared__ float row[D]; __shared__ float2 tw[D];
  for(int d=threadIdx.x;d<D;d+=blockDim.x){ row[d]=h[s*D+d]; float sn,cs; sincospif(2.f*d/D,&sn,&cs); tw[d]=make_float2(cs,-sn);} __syncthreads();
  for(int k=threadIdx.x;k<DF;k+=blockDim.x){ float re=0,im=0; int idx=0; for(int d=0;d<D;d++){ re+=row[d]*tw[idx].x; im+=row[d]*tw[idx].y; idx+=k; if(idx>=D) idx-=D;} R[s*DF+k]=make_float2(re,im);} }
__global__ void k_coldft(const float2* in, float2* out, float sgn, const u16* wr, const u16* wi){
  int k=blockIdx.x; __shared__ float2 col[S]; __shared__ float2 tw[S];
  for(int s=threadIdx.x;s<S;s+=blockDim.x){ col[s]=in[s*DF+k]; float sn,cs; sincospif(2.f*s/S,&sn,&cs); tw[s]=make_float2(cs,sgn*sn);} __syncthreads();
  for(int q=threadIdx.x;q<S;q+=blockDim.x){ float re=0,im=0; int idx=0; for(int s=0;s<S;s++){ float2 a=col[s],w=tw[idx]; re+=a.x*w.x-a.y*w.y; im+=a.x*w.y+a.y*w.x; idx=(idx+q)&(S-1);}
    if(wr){ float a=bf2f(wr[q*DF+k]), b=bf2f(wi[q*DF+k]); float r2=re*a-im*b, i2=re*b+im*a; re=r2; im=i2; }
    out[q*DF+k]=make_float2(re,im);} }
__global__ void k_rowidft(const float2* G, const float* x, const float* ga, float* x1){
  int s=blockIdx.x; __shared__ float2 row[DF]; __shared__ float2 tw[D];
  for(int d=threadIdx.x;d<D;d+=blockDim.x){ if(d<DF) row[d]=G[s*DF+d]; float sn,cs; sincospif(2.f*d/D,&sn,&cs); tw[d]=make_float2(cs,sn);} __syncthreads();
  for(int d=threadIdx.x;d<D;d+=blockDim.x){ float acc=0; int idx=d; for(int k=1;k<320;k++){ acc+=row[k].x*tw[idx].x-row[k].y*tw[idx].y; idx+=d; if(idx>=D) idx-=D;}
    acc=2.f*acc+row[0].x+((d&1)?-row[320].x:row[320].x); float y=rbf(acc/(float)(S*D)); x1[s*D+d]=rbf(x[s*D+d]+rbf(ga[d]*y)); } }
__global__ void k_gemm1(const float* h, const u16* wg, const u16* wu, float* hh){
  int s=blockIdx.x; __shared__ float row[D]; for(int d=threadIdx.x;d<D;d+=blockDim.x) row[d]=h[s*D+d]; __syncthreads();
  for(int f=threadIdx.x;f<F;f+=blockDim.x){ float g=0,u=0; for(int d=0;d<D;d++){ g+=row[d]*bf2f(wg[d*F+f]); u+=row[d]*bf2f(wu[d*F+f]); } g=rbf(g); u=rbf(u); g=g>0?g:0.f; hh[s*F+f]=rbf(g*u);} }
__global__ void k_gemm2(const float* hh, const u16* wd, const float* x1, const float* gb, u16* out){
  int s=blockIdx.x; __shared__ float row[F]; for(int d=threadIdx.x;d<F;d+=blockDim.x) row[d]=hh[s*F+d]; __syncthreads();
  for(int d=threadIdx.x;d<D;d+=blockDim.x){ float o=0; for(int f=0;f<F;f++) o+=row[f]*bf2f(wd[f*D+d]); o=rbf(o); out[s*D+d]=f2bf(x1[s*D+d]+rbf(gb[d]*o)); } }

extern "C" void kernel_launch(void* const* d_in, const int* in_sizes, int n_in, void* d_out, int out_size, void* d_ws, size_t ws_size, hipStream_t stream){
  const u16* x=(const u16*)d_in[0]; const u16* wr=(const u16*)d_in[1]; const u16* wi=(const u16*)d_in[2];
  const u16* wg=(const u16*)d_in[4]; const u16* wu=(const u16*)d_in[5]; const u16* wd=(const u16*)d_in[6]; const u16* c=(const u16*)d_in[7]; const u16* wm=(const u16*)d_in[8];
  char* ws=(char*)d_ws; float* mod=(float*)ws; float* xf=(float*)(ws+(1<<20)); float* h=(float*)(ws+(8<<20)); float2* R=(float2*)(ws+(16<<20)); float2* R2=(float2*)(ws+(24<<20)); float* x1=(float*)(ws+(32<<20)); float* hh=(float*)(ws+(40<<20));
  k_mod<<<15,256,0,stream>>>(c,wm,mod);
  k_cvt<<<S*D/256,256,0,stream>>>(x,xf);
  k_ln<<<S,256,0,stream>>>(xf,mod,mod+D,h);
  k_rowdft<<<S,320,0,stream>>>(h,R);
  k_coldft<<<DF,1024,0,stream>>>(R,R2,-1.f,wr,wi);
  k_coldft<<<DF,1024,0,stream>>>(R2,R,1.f,nullptr,nullptr);
  k_rowidft<<<S,320,0,stream>>>(R,xf,mod+2*D,x1);
  k_ln<<<S,256,0,stream>>>(x1,mod+3*D,mod+4*D,h);
  k_gemm1<<<S,256,0,stream>>>(h,wg,wu,hh);
  k_gemm2<<<S,320,0,stream>>>(hh,wd,x1,mod+5*D,(u16*)d_out);
}
```

```cpp
#include <hip/hip_runtime.h>
#include <hip/hip_bf16.h>
#include <stdint.h>

typedef unsigned u32;
typedef unsigned short u16;
typedef u32 u32x4 __attribute__((ext_vector_type(4)));
typedef u32 u32x2 __attribute__((ext_vector_type(2)));
typedef u32 u32x4a4 __attribute__((ext_vector_type(4), aligned(4)));
typedef u32 u32x4a8 __attribute__((ext_vector_type(4), aligned(8)));
typedef u32 u32x2a2 __attribute__((ext_vector_type(2), aligned(2)));
typedef float f32x16 __attribute__((ext_vector_type(16)));
typedef float f32x4 __attribute__((ext_vector_type(4)));
typedef float f32x2 __attribute__((ext_vector_type(2)));
typedef _Float16 f16x2 __attribute__((ext_vector_type(2)));
typedef __bf16 bf16x2 __attribute__((ext_vector_type(2)));
typedef __amdgpu_buffer_rsrc_t rsrc_t;
#define LAS __attribute__((address_space(3)))
#define DI __device__ __forceinline__
#define MEMFENCE() asm volatile("" ::: "memory")
#define LBAR() asm volatile("s_waitcnt lgkmcnt(0)\n s_barrier" ::: "memory")

static __device__ const unsigned TAB[4608] __attribute__((aligned(128))) = {
0x31a8,0x31a8,0x31a8,0x31a8,0x31a8,0x286a318c,0x2c54313a,0x2e4930b4,0x31a8,0x2c54313a,0x30003000,0x313a2c54,
0x31a8,0x2e4930b4,0x313a2c54,0x318ca86a,0x31a8,0x30003000,0x31a80000,0x3000b000,0x31a8,0x30b42e49,0x313aac54,0x286ab18c,
0x31a8,0x313a2c54,0x3000b000,0xac54b13a,0x31a8,0x318c286a,0x2c54b13a,0xb0b4ae49,0x31a8,0x31a80000,0xb1a8,0xb1a88000,
0x31a8,0x318ca86a,0xac54b13a,0xb0b42e49,0x31a8,0x313aac54,0xb000b000,0xac54313a,0x31a8,0x30b4ae49,0xb13aac54,0x286a318c,
0x31a8,0x3000b000,0xb1a88000,0x30003000,0x31a8,0x2e49b0b4,0xb13a2c54,0x318c286a,0x31a8,0x2c54b13a,0xb0003000,0x313aac54,
0x31a8,0x286ab18c,0xac54313a,0x2e49b0b4,0x31a8,0xb1a8,0x31a8,0xb1a8,0x31a8,0xa86ab18c,0x2c54313a,0xae49b0b4,
0x31a8,0xac54b13a,0x30003000,0xb13aac54,0x31a8,0xae49b0b4,0x313a2c54,0xb18c286a,0x31a8,0xb000b000,0x31a80000,0xb0003000,
0x31a8,0xb0b4ae49,0x313aac54,0xa86a318c,0x31a8,0xb13aac54,0x3000b000,0x2c54313a,0x31a8,0xb18ca86a,0x2c54b13a,0x30b42e49,
0x31a8,0xb1a88000,0xb1a8,0x31a80000,0x31a8,0xb18c286a,0xac54b13a,0x30b4ae49,0x31a8,0xb13a2c54,0xb000b000,0x2c54b13a,
0x31a8,0xb0b42e49,0xb13aac54,0xa86ab18c,0x31a8,0xb0003000,0xb1a88000,0xb000b000,0x31a8,0xae4930b4,0xb13a2c54,0xb18ca86a,
0x31a8,0xac54313a,0xb0003000,0xb13a2c54,0x31a8,0xa86a318c,0xac54313a,0xae4930b4,0x31a8,0x31a8,0x31a8,0x31a8,
0x30003000,0x30b42e49,0x313a2c54,0x318c286a,0x31a80000,0x313aac54,0x3000b000,0x2c54b13a,0x3000b000,0x286ab18c,0xac54b13a,0xb0b4ae49,
0xb1a8,0xb000b000,0xb1a88000,0xb0003000,0xb000b000,0xb18c286a,0xac54313a,0x2e4930b4,0xb1a88000,0xac54313a,0x30003000,0x313aac54,
0xb0003000,0x2e4930b4,0x313aac54,0xa86ab18c,0x31a8,0x31a80000,0xb1a8,0xb1a88000,0x30003000,0x2e49b0b4,0xb13aac54,0xa86a318c,
0x31a80000,0xac54b13a,0xb0003000,0x313a2c54,0x3000b000,0xb18ca86a,0x2c54313a,0x2e49b0b4,0xb1a8,0xb0003000,0x31a80000,0xb000b000,
0xb000b000,0x286a318c,0x2c54b13a,0xb0b42e49,0xb1a88000,0x313a2c54,0xb000b000,0x2c54313a,0xb0003000,0x30b4ae49,0xb13a2c54,0x318ca86a,
0x31a8,0xb1a8,0x31a8,0xb1a8,0x30003000,0xb0b4ae49,0x313a2c54,0xb18ca86a,0x31a80000,0xb13a2c54,0x3000b000,0xac54313a,
0x3000b000,0xa86a318c,0xac54b13a,0x30b42e49,0xb1a8,0x30003000,0xb1a88000,0x3000b000,0xb000b000,0x318ca86a,0xac54313a,0xae49b0b4,
0xb1a88000,0x2c54b13a,0x30003000,0xb13a2c54,0xb0003000,0xae49b0b4,0x313aac54,0x286a318c,0x31a8,0xb1a88000,0xb1a8,0x31a80000,
0x30003000,0xae4930b4,0xb13aac54,0x286ab18c,0x31a80000,0x2c54313a,0xb0003000,0xb13aac54,0x3000b000,0x318c286a,0x2c54313a,0xae4930b4,
0xb1a8,0x3000b000,0x31a80000,0x30003000,0xb000b000,0xa86ab18c,0x2c54b13a,0x30b4ae49,0xb1a88000,0xb13aac54,0xb000b000,0xac54b13a,
0xb0003000,0xb0b42e49,0xb13a2c54,0xb18c286a,0x31a8,0x31a8,0x31a8,0x31a8,0x31a80000,0x318ca86a,0x313aac54,0x30b4ae49,
0xb1a8,0xac54b13a,0xb000b000,0xb13aac54,0xb1a88000,0xb0b42e49,0xac54313a,0x286a318c,0x31a8,0x30003000,0x31a80000,0x3000b000,
0x31a80000,0x2e49b0b4,0xac54b13a,0xb18ca86a,0xb1a8,0xb13aac54,0xb0003000,0x2c54313a,0xb1a88000,0xa86a318c,0x313a2c54,0x2e49b0b4,
0x31a8,0x31a80000,0xb1a8,0xb1a88000,0x31a80000,0xa86ab18c,0xb13a2c54,0x2e4930b4,0xb1a8,0xb13a2c54,0x30003000,0x2c54b13a,
0xb1a88000,0x2e4930b4,0x2c54b13a,0xb18c286a,0x31a8,0x3000b000,0xb1a88000,0x30003000,0x31a80000,0xb0b4ae49,0x2c54313a,0x286ab18c,
0xb1a8,0xac54313a,0x3000b000,0xb13a2c54,0xb1a88000,0x318c286a,0xb13aac54,0x30b42e49,0x31a8,0xb1a8,0x31a8,0xb1a8,
0x31a80000,0xb18c286a,0x313aac54,0xb0b42e49,0xb1a8,0x2c54313a,0xb000b000,0x313a2c54,0xb1a88000,0x30b4ae49,0xac54313a,0xa86ab18c,
0x31a8,0xb000b000,0x31a80000,0xb0003000,0x31a80000,0xae4930b4,0xac54b13a,0x318c286a,0xb1a8,0x313a2c54,0xb0003000,0xac54b13a,
0xb1a88000,0x286ab18c,0x313a2c54,0xae4930b4,0x31a8,0xb1a88000,0xb1a8,0x31a80000,0x31a80000,0x286a318c,0xb13a2c54,0xae49b0b4,
0xb1a8,0x313aac54,0x30003000,0xac54313a,0xb1a88000,0xae49b0b4,0x2c54b13a,0x318ca86a,0x31a8,0xb0003000,0xb1a88000,0xb000b000,
0x31a80000,0x30b42e49,0x2c54313a,0xa86a318c,0xb1a8,0x2c54b13a,0x3000b000,0x313aac54,0xb1a88000,0xb18ca86a,0xb13aac54,0xb0b4ae49,
0x31a8,0x31a8,0x31a8,0x31a8,0x3000b000,0x2e49b0b4,0x2c54b13a,0x286ab18c,0xb1a88000,0xb13a2c54,0xb0003000,0xac54313a,
0x30003000,0x318c286a,0x313aac54,0x2e49b0b4,0xb1a8,0xb000b000,0xb1a88000,0xb0003000,0xb0003000,0x286a318c,0x313a2c54,0x30b4ae49,
0x31a80000,0x2c54b13a,0xb000b000,0xb13a2c54,0xb000b000,0xb0b42e49,0x2c54313a,0x318ca86a,0x31a8,0x31a80000,0xb1a8,0xb1a88000,
0x3000b000,0xb0b4ae49,0xac54313a,0x318c286a,0xb1a88000,0x2c54313a,0x3000b000,0xb13aac54,0x30003000,0x286ab18c,0xb13a2c54,0x30b42e49,
0xb1a8,0xb0003000,0x31a80000,0xb000b000,0xb0003000,0x318ca86a,0xb13aac54,0x2e4930b4,0x31a80000,0xb13aac54,0x30003000,0xac54b13a,
0xb000b000,0x2e4930b4,0xac54b13a,0x286a318c,0x31a8,0xb1a8,0x31a8,0xb1a8,0x3000b000,0xae4930b4,0x2c54b13a,0xa86a318c,
0xb1a88000,0x313aac54,0xb0003000,0x2c54b13a,0x30003000,0xb18ca86a,0x313aac54,0xae4930b4,0xb1a8,0x30003000,0xb1a88000,0x3000b000,
0xb0003000,0xa86ab18c,0x313a2c54,0xb0b42e49,0x31a80000,0xac54313a,0xb000b000,0x313aac54,0xb000b000,0x30b4ae49,0x2c54313a,0xb18c286a,
0x31a8,0xb1a88000,0xb1a8,0x31a80000,0x3000b000,0x30b42e49,0xac54313a,0xb18ca86a,0xb1a88000,0xac54b13a,0x3000b000,0x313a2c54,
0x30003000,0xa86a318c,0xb13a2c54,0xb0b4ae49,0xb1a8,0x3000b000,0x31a80000,0x30003000,0xb0003000,0xb18c286a,0xb13aac54,0xae49b0b4,
0x31a80000,0x313a2c54,0x30003000,0x2c54313a,0xb000b000,0xae49b0b4,0xac54b13a,0xa86ab18c,0x31a8,0x31a8,0x31a8,0x31a8,
0xb1a8,0xa86ab18c,0xac54b13a,0xae49b0b4,0x31a8,0x2c54313a,0x30003000,0x313a2c54,0xb1a8,0xae49b0b4,0xb13aac54,0xb18c286a,
0x31a8,0x30003000,0x31a80000,0x3000b000,0xb1a8,0xb0b4ae49,0xb13a2c54,0xa86a318c,0x31a8,0x313a2c54,0x3000b000,0xac54b13a,
0xb1a8,0xb18ca86a,0xac54313a,0x30b42e49,0x31a8,0x31a80000,0xb1a8,0xb1a88000,0xb1a8,0xb18c286a,0x2c54313a,0x30b4ae49,
0x31a8,0x313aac54,0xb000b000,0xac54313a,0xb1a8,0xb0b42e49,0x313a2c54,0xa86ab18c,0x31a8,0x3000b000,0xb1a88000,0x30003000,
0xb1a8,0xae4930b4,0x313aac54,0xb18ca86a,0x31a8,0x2c54b13a,0xb0003000,0x313aac54,0xb1a8,0xa86a318c,0x2c54b13a,0xae4930b4,
0x31a8,0xb1a8,0x31a8,0xb1a8,0xb1a8,0x286a318c,0xac54b13a,0x2e4930b4,0x31a8,0xac54b13a,0x30003000,0xb13aac54,
0xb1a8,0x2e4930b4,0xb13aac54,0x318ca86a,0x31a8,0xb000b000,0x31a80000,0xb0003000,0xb1a8,0x30b42e49,0xb13a2c54,0x286ab18c,
0x31a8,0xb13aac54,0x3000b000,0x2c54313a,0xb1a8,0x318c286a,0xac54313a,0xb0b4ae49,0x31a8,0xb1a88000,0xb1a8,0x31a80000,
0xb1a8,0x318ca86a,0x2c54313a,0xb0b42e49,0x31a8,0xb13a2c54,0xb000b000,0x2c54b13a,0xb1a8,0x30b4ae49,0x313a2c54,0x286a318c,
0x31a8,0xb0003000,0xb1a88000,0xb000b000,0xb1a8,0x2e49b0b4,0x313aac54,0x318c286a,0x31a8,0xac54313a,0xb0003000,0xb13a2c54,
0xb1a8,0x286ab18c,0x2c54b13a,0x2e49b0b4,0x31a8,0x31a8,0x31a8,0x31a8,0xb000b000,0xb0b4ae49,0xb13aac54,0xb18ca86a,
0x31a80000,0x313aac54,0x3000b000,0x2c54b13a,0xb0003000,0xa86a318c,0x2c54313a,0x30b42e49,0xb1a8,0xb000b000,0xb1a88000,0xb0003000,
0x30003000,0x318ca86a,0x2c54b13a,0xae49b0b4,0xb1a88000,0xac54313a,0x30003000,0x313aac54,0x3000b000,0xae49b0b4,0xb13a2c54,0x286a318c,
0x31a8,0x31a80000,0xb1a8,0xb1a88000,0xb000b000,0xae4930b4,0x313a2c54,0x286ab18c,0x31a80000,0xac54b13a,0xb0003000,0x313a2c54,
0xb0003000,0x318c286a,0xac54b13a,0xae4930b4,0xb1a8,0xb0003000,0x31a80000,0xb000b000,0x30003000,0xa86ab18c,0xac54313a,0x30b4ae49,
0xb1a88000,0x313a2c54,0xb000b000,0x2c54313a,0x3000b000,0xb0b42e49,0x313aac54,0xb18c286a,0x31a8,0xb1a8,0x31a8,0xb1a8,
0xb000b000,0x30b42e49,0xb13aac54,0x318c286a,0x31a80000,0xb13a2c54,0x3000b000,0xac54313a,0xb0003000,0x286ab18c,0x2c54313a,0xb0b4ae49,
0xb1a8,0x30003000,0xb1a88000,0x3000b000,0x30003000,0xb18c286a,0x2c54b13a,0x2e4930b4,0xb1a88000,0x2c54b13a,0x30003000,0xb13a2c54,
0x3000b000,0x2e4930b4,0xb13a2c54,0xa86ab18c,0x31a8,0xb1a88000,0xb1a8,0x31a80000,0xb000b000,0x2e49b0b4,0x313a2c54,0xa86a318c,
0x31a80000,0x2c54313a,0xb0003000,0xb13aac54,0xb0003000,0xb18ca86a,0xac54b13a,0x2e49b0b4,0xb1a8,0x3000b000,0x31a80000,0x30003000,
0x30003000,0x286a318c,0xac54313a,0xb0b42e49,0xb1a88000,0xb13aac54,0xb000b000,0xac54b13a,0x3000b000,0x30b4ae49,0x313aac54,0x318ca86a,
0x31a8,0x31a8,0x31a8,0x31a8,0xb1a88000,0xb18c286a,0xb13a2c54,0xb0b42e49,0xb1a8,0xac54b13a,0xb000b000,0xb13aac54,
0x31a80000,0x30b4ae49,0x2c54b13a,0xa86ab18c,0x31a8,0x30003000,0x31a80000,0x3000b000,0xb1a88000,0xae4930b4,0x2c54313a,0x318c286a,
0xb1a8,0xb13aac54,0xb0003000,0x2c54313a,0x31a80000,0x286ab18c,0xb13aac54,0xae4930b4,0x31a8,0x31a80000,0xb1a8,0xb1a88000,
0xb1a88000,0x286a318c,0x313aac54,0xae49b0b4,0xb1a8,0xb13a2c54,0x30003000,0x2c54b13a,0x31a80000,0xae49b0b4,0xac54313a,0x318ca86a,
0x31a8,0x3000b000,0xb1a88000,0x30003000,0xb1a88000,0x30b42e49,0xac54b13a,0xa86a318c,0xb1a8,0xac54313a,0x3000b000,0xb13a2c54,
0x31a80000,0xb18ca86a,0x313a2c54,0xb0b4ae49,0x31a8,0xb1a8,0x31a8,0xb1a8,0xb1a88000,0x318ca86a,0xb13a2c54,0x30b4ae49,
0xb1a8,0x2c54313a,0xb000b000,0x313a2c54,0x31a80000,0xb0b42e49,0x2c54b13a,0x286a318c,0x31a8,0xb000b000,0x31a80000,0xb0003000,
0xb1a88000,0x2e49b0b4,0x2c54313a,0xb18ca86a,0xb1a8,0x313a2c54,0xb0003000,0xac54b13a,0x31a80000,0xa86a318c,0xb13aac54,0x2e49b0b4,
0x31a8,0xb1a88000,0xb1a8,0x31a80000,0xb1a88000,0xa86ab18c,0x313aac54,0x2e4930b4,0xb1a8,0x313aac54,0x30003000,0xac54313a,
0x31a80000,0x2e4930b4,0xac54313a,0xb18c286a,0x31a8,0xb0003000,0xb1a88000,0xb000b000,0xb1a88000,0xb0b4ae49,0xac54b13a,0x286ab18c,
0xb1a8,0x2c54b13a,0x3000b000,0x313aac54,0x31a80000,0x318c286a,0x313a2c54,0x30b42e49,0x31a8,0x31a8,0x31a8,0x31a8,
0xb0003000,0xae4930b4,0xac54313a,0xa86a318c,0xb1a88000,0xb13a2c54,0xb0003000,0xac54313a,0xb000b000,0xb18ca86a,0xb13a2c54,0xae4930b4,
0xb1a8,0xb000b000,0xb1a88000,0xb0003000,0x3000b000,0xa86ab18c,0xb13aac54,0xb0b42e49,0x31a80000,0x2c54b13a,0xb000b000,0xb13a2c54,
0x30003000,0x30b4ae49,0xac54b13a,0xb18c286a,0x31a8,0x31a80000,0xb1a8,0xb1a88000,0xb0003000,0x30b42e49,0x2c54b13a,0xb18ca86a,
0xb1a88000,0x2c54313a,0x3000b000,0xb13aac54,0xb000b000,0xa86a318c,0x313aac54,0xb0b4ae49,0xb1a8,0xb0003000,0x31a80000,0xb000b000,
0x3000b000,0xb18c286a,0x313a2c54,0xae49b0b4,0x31a80000,0xb13aac54,0x30003000,0xac54b13a,0x30003000,0xae49b0b4,0x2c54313a,0xa86ab18c,
0x31a8,0xb1a8,0x31a8,0xb1a8,0xb0003000,0x2e49b0b4,0xac54313a,0x286ab18c,0xb1a88000,0x313aac54,0xb0003000,0x2c54b13a,
0xb000b000,0x318c286a,0xb13a2c54,0x2e49b0b4,0xb1a8,0x30003000,0xb1a88000,0x3000b000,0x3000b000,0x286a318c,0xb13aac54,0x30b4ae49,
0x31a80000,0xac54313a,0xb000b000,0x313aac54,0x30003000,0xb0b42e49,0xac54b13a,0x318ca86a,0x31a8,0xb1a88000,0xb1a8,0x31a80000,
0xb0003000,0xb0b4ae49,0x2c54b13a,0x318c286a,0xb1a88000,0xac54b13a,0x3000b000,0x313a2c54,0xb000b000,0x286ab18c,0x313aac54,0x30b42e49,
0xb1a8,0x3000b000,0x31a80000,0x30003000,0x3000b000,0x318ca86a,0x313a2c54,0x2e4930b4,0x31a80000,0x313a2c54,0x30003000,0x2c54313a,
0x30003000,0x2e4930b4,0x2c54313a,0x286a318c,0x3328,0x3328,0x3328,0x3328,0x3328,0x2c6c32ce,0x303531ca,0x31ca3035,
0x3328,0x303531ca,0x32ce2c6c,0x32ceac6c,0x3328,0x31ca3035,0x32ceac6c,0x2c6cb2ce,0x3328,0x32ce2c6c,0x3035b1ca,0xb035b1ca,
0x3328,0x33280000,0xb328,0xb3288000,0x3328,0x32ceac6c,0xb035b1ca,0xb03531ca,0x3328,0x31cab035,0xb2ceac6c,0x2c6c32ce,
0x3328,0x3035b1ca,0xb2ce2c6c,0x32ce2c6c,0x3328,0x2c6cb2ce,0xb03531ca,0x31cab035,0x3328,0xb328,0x3328,0xb328,
0x3328,0xac6cb2ce,0x303531ca,0xb1cab035,0x3328,0xb035b1ca,0x32ce2c6c,0xb2ce2c6c,0x3328,0xb1cab035,0x32ceac6c,0xac6c32ce,
0x3328,0xb2ceac6c,0x3035b1ca,0x303531ca,0x3328,0xb3288000,0xb328,0x33280000,0x3328,0xb2ce2c6c,0xb035b1ca,0x3035b1ca,
0x3328,0xb1ca3035,0xb2ceac6c,0xac6cb2ce,0x0,0x0,0x0,0x0,0x0,0x0,0x0,0x0,
0x3328,0xb03531ca,0xb2ce2c6c,0xb2ceac6c,0x3328,0xac6c32ce,0xb03531ca,0xb1ca3035,0x0,0x0,0x0,0x0,
0x0,0x0,0x0,0x0,0x0,0x0,0x0,0x0,0x0,0x0,0x0,0x0,
0x0,0x0,0x0,0x0,0x0,0x0,0x0,0x0,0x0,0x0,0x0,0x0,
0x0,0x0,0x0,0x0,0x0,0x0,0x0,0x0,0x0,0x0,0x0,0x0,
0x3328,0x3328,0x3328,0x3328,0x32ce2c6c,0x33280000,0x32ceac6c,0x31cab035,0x3035b1ca,0xb328,0xb035b1ca,0xb2ceac6c,
0xb035b1ca,0xb3288000,0xb03531ca,0x2c6c32ce,0xb2ce2c6c,0x3328,0x32ce2c6c,0x3035b1ca,0x3328,0x33280000,0xb328,0xb3288000,
0x32ce2c6c,0xb328,0xb2ce2c6c,0x303531ca,0x3035b1ca,0xb3288000,0x303531ca,0x2c6cb2ce,0xb035b1ca,0x3328,0x3035b1ca,0xb2ce2c6c,
0xb2ce2c6c,0x33280000,0xb2ceac6c,0x31ca3035,0x3328,0xb328,0x3328,0xb328,0x32ce2c6c,0xb3288000,0x32ceac6c,0xb1ca3035,
0x3035b1ca,0x3328,0xb035b1ca,0x32ce2c6c,0xb035b1ca,0x33280000,0xb03531ca,0xac6cb2ce,0xb2ce2c6c,0xb328,0x32ce2c6c,0xb03531ca,
0x3328,0xb3288000,0xb328,0x33280000,0x32ce2c6c,0x3328,0xb2ce2c6c,0xb035b1ca,0x3035b1ca,0x33280000,0x303531ca,0xac6c32ce,
0x0,0x0,0x0,0x0,0x0,0x0,0x0,0x0,0xb035b1ca,0xb328,0x3035b1ca,0x32ceac6c,
0xb2ce2c6c,0xb3288000,0xb2ceac6c,0xb1cab035,0x0,0x0,0x0,0x0,0x0,0x0,0x0,0x0,
0x0,0x0,0x0,0x0,0x0,0x0,0x0,0x0,0x0,0x0,0x0,0x0,
0x0,0x0,0x0,0x0,0x0,0x0,0x0,0x0,0x0,0x0,0x0,0x0,
0x0,0x0,0x0,0x0,0x0,0x0,0x0,0x0,0x3328,0x3328,0x3328,0x3328,
0x3035b1ca,0x2c6cb2ce,0xb328,0xac6cb2ce,0xb2ce2c6c,0xb03531ca,0x3328,0x303531ca,0x32ce2c6c,0x31cab035,0xb328,0xb1cab035,
0xb035b1ca,0xb2ce2c6c,0x3328,0x32ce2c6c,0x3328,0x33280000,0xb328,0xb3288000,0x3035b1ca,0xb2ceac6c,0x3328,0x32ceac6c,
0xb2ce2c6c,0x31ca3035,0xb328,0xb1ca3035,0x32ce2c6c,0xb035b1ca,0x3328,0x3035b1ca,0xb035b1ca,0x2c6c32ce,0xb328,0xac6c32ce,
0x3328,0xb328,0x3328,0xb328,0x3035b1ca,0xac6c32ce,0xb328,0x2c6c32ce,0xb2ce2c6c,0x3035b1ca,0x3328,0xb035b1ca,
0x32ce2c6c,0xb1ca3035,0xb328,0x31ca3035,0xb035b1ca,0x32ceac6c,0x3328,0xb2ceac6c,0x3328,0xb3288000,0xb328,0x33280000,
0x3035b1ca,0x32ce2c6c,0x3328,0xb2ce2c6c,0xb2ce2c6c,0xb1cab035,0xb328,0x31cab035,0x0,0x0,0x0,0x0,
0x0,0x0,0x0,0x0,0x32ce2c6c,0x303531ca,0x3328,0xb03531ca,0xb035b1ca,0xac6cb2ce,0xb328,0x2c6cb2ce,
0x0,0x0,0x0,0x0,0x0,0x0,0x0,0x0,0x0,0x0,0x0,0x0,
0x0,0x0,0x0,0x0,0x0,0x0,0x0,0x0,0x0,0x0,0x0,0x0,
0x0,0x0,0x0,0x0,0x0,0x0,0x0,0x0,0x0,0x0,0x0,0x0,
0x0,0x0,0x0,0x0,0x3328,0x3328,0x3328,0x3328,0xb035b1ca,0xb1cab035,0xb2ceac6c,0xb3288000,
0x32ce2c6c,0x32ceac6c,0x3035b1ca,0xb328,0xb2ce2c6c,0xac6c32ce,0x303531ca,0x33280000,0x3035b1ca,0xb035b1ca,0xb2ce2c6c,0x3328,
0x3328,0x33280000,0xb328,0xb3288000,0xb035b1ca,0xb03531ca,0x32ce2c6c,0xb328,0x32ce2c6c,0xac6cb2ce,0xb03531ca,0x33280000,
0xb2ce2c6c,0x32ce2c6c,0xb035b1ca,0x3328,0x3035b1ca,0xb1ca3035,0x32ceac6c,0xb3288000,0x3328,0xb328,0x3328,0xb328,
0xb035b1ca,0x31ca3035,0xb2ceac6c,0x33280000,0x32ce2c6c,0xb2ce2c6c,0x3035b1ca,0x3328,0xb2ce2c6c,0x2c6cb2ce,0x303531ca,0xb3288000,
0x3035b1ca,0x303531ca,0xb2ce2c6c,0xb328,0x3328,0xb3288000,0xb328,0x33280000,0xb035b1ca,0x3035b1ca,0x32ce2c6c,0x3328,
0x32ce2c6c,0x2c6c32ce,0xb03531ca,0xb3288000,0x0,0x0,0x0,0x0,0x0,0x0,0x0,0x0,
0xb2ce2c6c,0xb2ceac6c,0xb035b1ca,0xb328,0x3035b1ca,0x31cab035,0x32ceac6c,0x33280000,0x0,0x0,0x0,0x0,
0x0,0x0,0x0,0x0,0x0,0x0,0x0,0x0,0x0,0x0,0x0,0x0,
0x0,0x0,0x0,0x0,0x0,0x0,0x0,0x0,0x0,0x0,0x0,0x0,
0x0,0x0,0x0,0x0,0x0,0x0,0x0,0x0,0x0,0x0,0x0,0x0,
0x3328,0x3328,0x0,0x0,0xb2ce2c6c,0xb1ca3035,0x0,0x0,0xb035b1ca,0xb2ceac6c,0x0,0x0,
0x3035b1ca,0xac6cb2ce,0x0,0x0,0x32ce2c6c,0x3035b1ca,0x0,0x0,0x3328,0x33280000,0x0,0x0,
0xb2ce2c6c,0x303531ca,0x0,0x0,0xb035b1ca,0xac6c32ce,0x0,0x0,0x3035b1ca,0xb2ce2c6c,0x0,0x0,
0x32ce2c6c,0xb1cab035,0x0,0x0,0x3328,0xb328,0x0,0x0,0xb2ce2c6c,0x31cab035,0x0,0x0,
0xb035b1ca,0x32ce2c6c,0x0,0x0,0x3035b1ca,0x2c6c32ce,0x0,0x0,0x32ce2c6c,0xb03531ca,0x0,0x0,
0x3328,0xb3288000,0x0,0x0,0xb2ce2c6c,0xb035b1ca,0x0,0x0,0xb035b1ca,0x2c6cb2ce,0x0,0x0,
0x0,0x0,0x0,0x0,0x0,0x0,0x0,0x0,0x3035b1ca,0x32ceac6c,0x0,0x0,
0x32ce2c6c,0x31ca3035,0x0,0x0,0x0,0x0,0x0,0x0,0x0,0x0,0x0,0x0,
0x0,0x0,0x0,0x0,0x0,0x0,0x0,0x0,0x0,0x0,0x0,0x0,
0x0,0x0,0x0,0x0,0x0,0x0,0x0,0x0,0x0,0x0,0x0,0x0,
0x0,0x0,0x0,0x0,0x0,0x0,0x0,0x0,0x3328,0x3328,0x0,0x0,
0xb03531ca,0xac6c32ce,0x0,0x0,0xb2ce2c6c,0xb03531ca,0x0,0x0,0xb2ceac6c,0xb1ca3035,0x0,0x0,
0xb035b1ca,0xb2ce2c6c,0x0,0x0,0xb328,0xb3288000,0x0,0x0,0x3035b1ca,0xb2ceac6c,0x0,0x0,
0x32ceac6c,0xb1cab035,0x0,0x0,0x32ce2c6c,0xb035b1ca,0x0,0x0,0x303531ca,0xac6cb2ce,0x0,0x0,
0x3328,0xb328,0x0,0x0,0xb03531ca,0x2c6cb2ce,0x0,0x0,0xb2ce2c6c,0x3035b1ca,0x0,0x0,
0xb2ceac6c,0x31cab035,0x0,0x0,0xb035b1ca,0x32ceac6c,0x0,0x0,0xb328,0x33280000,0x0,0x0,
0x3035b1ca,0x32ce2c6c,0x0,0x0,0x32ceac6c,0x31ca3035,0x0,0x0,0x0,0x0,0x0,0x0,
0x0,0x0,0x0,0x0,0x32ce2c6c,0x303531ca,0x0,0x0,0x303531ca,0x2c6c32ce,0x0,0x0,
0x0,0x0,0x0,0x0,0x0,0x0,0x0,0x0,0x0,0x0,0x0,0x0,
0x0,0x0,0x0,0x0,0x0,0x0,0x0,0x0,0x0,0x0,0x0,0x0,
0x0,0x0,0x0,0x0,0x0,0x0,0x0,0x0,0x0,0x0,0x0,0x0,
0x0,0x0,0x0,0x0,0x3c00,0x3c00,0x3c00,0x3c00,0x3c00,0x21073c00,0x25073c00,0x278a3bff,
0x3c00,0x25073c00,0x29063bfe,0x2b893bfc,0x3c00,0x278a3bff,0x2b893bfc,0x2da63bf8,0x3c00,0x29063bfe,0x2d053bfa,0x2f863bf2,
0x3c00,0x2a483bfe,0x2e463bf6,0x30b23bea,0x3c00,0x2b893bfc,0x2f863bf2,0x31a03be0,0x3c00,0x2c653bfb,0x30623bed,0x328d3bd5,
0x3c00,0x2d053bfa,0x31023be7,0x33783bc7,0x3c00,0x2da63bf8,0x31a03be0,0x34313bb8,0x3c00,0x2e463bf6,0x323e3bd9,0x34a53ba8,
0x3c00,0x2ee63bf4,0x32dc3bd0,0x35183b95,0x3c00,0x2f863bf2,0x33783bc7,0x358a3b81,0x3c00,0x30133bef,0x340a3bbe,0x35fa3b6c,
0x3c00,0x30623bed,0x34583bb3,0x36693b54,0x3c00,0x30b23bea,0x34a53ba8,0x36d73b3b,0x3c00,0x31023be7,0x34f23b9c,0x37443b21,
0x3c00,0x31513be4,0x353e3b8f,0x37ae3b05,0x3c00,0x31a03be0,0x358a3b81,0x380c3ae7,0x3c00,0x31ef3bdc,0x35d53b73,0x383f3ac8,
0x3c00,0x323e3bd9,0x361f3b64,0x38723aa7,0x3c00,0x328d3bd5,0x36693b54,0x38a33a85,0x3c00,0x32dc3bd0,0x36b33b44,0x38d43a61,
0x3c00,0x332a3bcc,0x36fc3b33,0x39043a3c,0x3c00,0x33783bc7,0x37443b21,0x39323a15,0x3c00,0x33c63bc3,0x378b3b0e,0x395f39ed,
0x3c00,0x340a3bbe,0x37d13afb,0x398b39c4,0x3c00,0x34313bb8,0x380c3ae7,0x39b6399a,0x3c00,0x34583bb3,0x382e3ad2,0x39e0396e,
0x3c00,0x347e3bae,0x38503abd,0x3a083941,0x3c00,0x34a53ba8,0x38723aa7,0x3a2f3913,0x3c00,0x34cb3ba2,0x38933a90,0x3a5538e4,
0x3c00,0x3c00,0x3c00,0x3c00,0x29063bfe,0x2a483bfe,0x2b893bfc,0x2c653bfb,0x2d053bfa,0x2e463bf6,0x2f863bf2,0x30623bed,
0x2f863bf2,0x30b23bea,0x31a03be0,0x328d3bd5,0x31023be7,0x323e3bd9,0x33783bc7,0x34583bb3,0x323e3bd9,0x33c63bc3,0x34a53ba8,0x35643b88,
0x33783bc7,0x34a53ba8,0x358a3b81,0x36693b54,0x34583bb3,0x35643b88,0x36693b54,0x37673b18,0x34f23b9c,0x361f3b64,0x37443b21,0x382e3ad2,
0x358a3b81,0x36d73b3b,0x380c3ae7,0x38a33a85,0x361f3b64,0x378b3b0e,0x38723aa7,0x39133a2f,0x36b33b44,0x381d3add,0x38d43a61,0x397d39d2,
0x37443b21,0x38723aa7,0x39323a15,0x39e0396e,0x37d13afb,0x38c43a6d,0x398b39c4,0x3a3c3904,0x382e3ad2,0x39133a2f,0x39e0396e,0x3a903893,
0x38723aa7,0x395f39ed,0x3a2f3913,0x3add381d,0x38b43a79,0x39a839a8,0x3a7938b4,0x3b213744,0x38f43a48,0x39ed395f,0x3abd3850,0x3b5c3645,
0x39323a15,0x3a2f3913,0x3afb37d1,0x3b8f353e,0x396e39e0,0x3a6d38c4,0x3b3336fc,0x3bb83431,0x39a839a8,0x3aa73872,0x3b64361f,0x3bd9323e,
0x39e0396e,0x3add381d,0x3b8f353e,0x3bef3013,0x3a153932,0x3b0e378b,0x3bb33458,0x3bfc2b89,0x3a4838f4,0x3b3b36d7,0x3bd032dc,0x3c00a107,
0x3a7938b4,0x3b64361f,0x3be73102,0x3bfaad05,0x3aa73872,0x3b883564,0x3bf62e46,0x3beab0b2,0x3ad2382e,0x3ba834a5,0x3bfe2906,0x3bd0b2dc,
0x3afb37d1,0x3bc333c6,0x3c00a507,0x3baeb47e,0x3b213744,0x3bd9323e,0x3bfaad05,0x3b81b58a,0x3b4436b3,0x3bea30b2,0x3bedb062,0x3b4cb68e,
0x3b64361f,0x3bf62e46,0x3bd9b23e,0x3b0eb78b,0x3b81358a,0x3bfe2a48,0x3bbeb40a,0x3ac8b83f,0x3c00,0x3c00,0x3c00,0x3c00,
0x2d053bfa,0x2da63bf8,0x2e463bf6,0x2ee63bf4,0x31023be7,0x31a03be0,0x323e3bd9,0x32dc3bd0,0x33783bc7,0x34313bb8,0x34a53ba8,0x35183b95,
0x34f23b9c,0x358a3b81,0x361f3b64,0x36b33b44,0x361f3b64,0x36d73b3b,0x378b3b0e,0x381d3add,0x37443b21,0x380c3ae7,0x38723aa7,0x38d43a61,
0x382e3ad2,0x38a33a85,0x39133a2f,0x397d39d2,0x38b43a79,0x39323a15,0x39a839a8,0x3a153932,0x39323a15,0x39b6399a,0x3a2f3913,0x3a9c3882,
0x39a839a8,0x3a2f3913,0x3aa73872,0x3b0e378b,0x3a153932,0x3a9c3882,0x3b0e378b,0x3b6c35fa,0x3a7938b4,0x3afb37d1,0x3b64361f,0x3bb33458,
0x3ad2382e,0x3b4c368e,0x3ba834a5,0x3be43151,0x3b213744,0x3b8f353e,0x3bd9323e,0x3bfc2b89,0x3b64361f,0x3bc333c6,0x3bf62e46,0x3bfeaa48,
0x3b9c34f2,0x3be73102,0x3c000000,0x3be7b102,0x3bc73378,0x3bfb2c65,0x3bf6ae46,0x3bb8b431,0x3be73102,0x3c00a507,0x3bd9b23e,0x3b73b5d5,
0x3bfa2d05,0x3bf4aee6,0x3ba8b4a5,0x3b18b767,0x3c000000,0x3bd9b23e,0x3b64b61f,0x3aa7b872,0x3bfaad05,0x3baeb47e,0x3b0eb78b,0x3a22b923,
0x3be7b102,0x3b73b5d5,0x3aa7b872,0x398bb9c4,0x3bc7b378,0x3b2ab720,0x3a2fb913,0x38e4ba55,0x3b9cb4f2,0x3ad2b82e,0x39a8b9a8,0x382ebad2,
0x3b64b61f,0x3a6db8c4,0x3913ba2f,0x36d7bb3b,0x3b21b744,0x39fbb950,0x3872baa7,0x353ebb8f,0x3ad2b82e,0x397db9d2,0x378bbb0e,0x332abbcc,
0x3a79b8b4,0x38f4ba48,0x361fbb64,0x2f86bbf2,0x3a15b932,0x3861bab2,0x34a5bba8,0x2107bc00,0x39a8b9a8,0x378bbb0e,0x323ebbd9,0xae46bbf6,
0x3932ba15,0x3645bb5c,0x2e46bbf6,0xb28dbbd5,0x3c00,0x3c00,0x3c00,0x3c00,0x2f863bf2,0x30133bef,0x30623bed,0x30b23bea,
0x33783bc7,0x340a3bbe,0x34583bb3,0x34a53ba8,0x358a3b81,0x35fa3b6c,0x36693b54,0x36d73b3b,0x37443b21,0x37d13afb,0x382e3ad2,0x38723aa7,
0x38723aa7,0x38c43a6d,0x39133a2f,0x395f39ed,0x39323a15,0x398b39c4,0x39e0396e,0x3a2f3913,0x39e0396e,0x3a3c3904,0x3a903893,0x3add381d,
0x3a7938b4,0x3ad2382e,0x3b213744,0x3b64361f,0x3afb37d1,0x3b4c368e,0x3b8f353e,0x3bc333c6,0x3b64361f,0x3ba834a5,0x3bd9323e,0x3bf62e46,
0x3bb33458,0x3be43151,0x3bfc2b89,0x3bfeaa48,0x3be73102,0x3bfe2906,0x3bfaad05,0x3bd9b23e,0x3bfe2906,0x3bf8ada6,0x3bd0b2dc,0x3b88b564,
0x3bfaad05,0x3bd0b2dc,0x3b81b58a,0x3b0eb78b,0x3bd9b23e,0x3b88b564,0x3b0eb78b,0x3a6db8c4,0x3b9cb4f2,0x3b21b744,0x3a79b8b4,0x39a8b9a8,
0x3b44b6b3,0x3a9cb882,0x39c4b98b,0x38c4ba6d,0x3ad2b82e,0x39fbb950,0x38f4ba48,0x378bbb0e,0x3a48b8f4,0x3941ba08,0x380cbae7,0x3564bb88,
0x39a8b9a8,0x3872baa7,0x361fbb64,0x323ebbd9,0x38f4ba48,0x3720bb2a,0x340abbbe,0x2a48bbfe,0x382ebad2,0x353ebb8f,0x2f86bbf2,0xae46bbf6,
0x36b3bb44,0x328dbbd5,0xa507bc00,0xb3c6bbc3,0x34f2bb9c,0x2d05bbfa,0xb102bbe7,0xb61fbb64,0x323ebbd9,0xaa48bbfe,0xb4a5bba8,0xb81dbadd,
0x2d05bbfa,0xb1a0bbe0,0xb6b3bb44,0xb913ba2f,0xa906bbfe,0xb4cbbba2,0xb850babd,0xb9edb95f,0xb102bbe7,0xb6b3bb44,0xb932ba15,0xbaa7b872,
0xb458bbb3,0xb83fbac8,0xb9fbb950,0xbb3bb6d7,0xb61fbb64,0xb913ba2f,0xbaa7b872,0xbba8b4a5,0xb7d1bafb,0xb9d2b97d,0xbb33b6fc,0xbbeab0b2,
0x3c00,0x3c00,0x0,0x0,0x31023be7,0x31513be4,0x0,0x0,0x34f23b9c,0x353e3b8f,0x0,0x0,
0x37443b21,0x37ae3b05,0x0,0x0,0x38b43a79,0x38f43a48,0x0,0x0,0x39a839a8,0x39ed395f,0x0,0x0,
0x3a7938b4,0x3abd3850,0x0,0x0,0x3b213744,0x3b5c3645,0x0,0x0,0x3b9c34f2,0x3bc73378,0x0,0x0,
0x3be73102,0x3bfb2c65,0x0,0x0,0x3c000000,0x3bf6ae46,0x0,0x0,0x3be7b102,0x3bb8b431,0x0,0x0,
0x3b9cb4f2,0x3b44b6b3,0x0,0x0,0x3b21b744,0x3a9cb882,0x0,0x0,0x3a79b8b4,0x39c4b98b,0x0,0x0,
0x39a8b9a8,0x38c4ba6d,0x0,0x0,0x38b4ba79,0x3744bb21,0x0,0x0,0x3744bb21,0x34cbbba2,0x0,0x0,
0x34f2bb9c,0x3062bbed,0x0,0x0,0x3102bbe7,0xa78abbff,0x0,0x0,0xbc00,0xb23ebbd9,0x0,0x0,
0xb102bbe7,0xb5afbb7a,0x0,0x0,0xb4f2bb9c,0xb80cbae7,0x0,0x0,0xb744bb21,0xb923ba22,0x0,0x0,
0xb8b4ba79,0xba15b932,0x0,0x0,0xb9a8b9a8,0xbaddb81d,0x0,0x0,0xba79b8b4,0xbb73b5d5,0x0,0x0,
0xbb21b744,0xbbd5b28d,0x0,0x0,0xbb9cb4f2,0xbbfea906,0x0,0x0,0xbbe7b102,0xbbef3013,0x0,0x0,
0xbc008000,0xbba834a5,0x0,0x0,0xbbe73102,0xbb2a3720,0x0,0x0,0x3c00,0x3c00,0x0,0x0,
0x31a03be0,0x31ef3bdc,0x0,0x0,0x358a3b81,0x35d53b73,0x0,0x0,0x380c3ae7,0x383f3ac8,0x0,0x0,
0x39323a15,0x396e39e0,0x0,0x0,0x3a2f3913,0x3a6d38c4,0x0,0x0,0x3afb37d1,0x3b3336fc,0x0,0x0,
0x3b8f353e,0x3bb83431,0x0,0x0,0x3be73102,0x3bfa2d05,0x0,0x0,0x3c00a507,0x3bf4aee6,0x0,0x0,
0x3bd9b23e,0x3ba8b4a5,0x0,0x0,0x3b73b5d5,0x3b18b767,0x0,0x0,0x3ad2b82e,0x3a48b8f4,0x0,0x0,
0x39fbb950,0x3941ba08,0x0,0x0,0x38f4ba48,0x380cbae7,0x0,0x0,0x378bbb0e,0x3564bb88,0x0,0x0,
0x34f2bb9c,0x3102bbe7,0x0,0x0,0x3062bbed,0xa78abbff,0x0,0x0,0xa906bbfe,0xb2dcbbd0,0x0,0x0,
0xb2dcbbd0,0xb645bb5c,0x0,0x0,0xb61fbb64,0xb872baa7,0x0,0x0,0xb850babd,0xb99ab9b6,0x0,0x0,
0xb96eb9e0,0xba90b893,0x0,0x0,0xba61b8d4,0xbb4cb68e,0x0,0x0,0xbb21b744,0xbbc7b378,0x0,0x0,
0xbba8b4a5,0xbbfeaa48,0x0,0x0,0xbbf2af86,0xbbed3062,0x0,0x0,0xbbfc2b89,0xbb953518,0x0,0x0,
0xbbc73378,0xbafb37d1,0x0,0x0,0xbb543669,0xba223923,0x0,0x0,0xbaa73872,0xb9133a2f,0x0,0x0,
0xb9c4398b,0xb7ae3b05,0x0,0x0,0x3c00,0x3c00,0x3c00,0x3c00,0x3c00,0x1e483c00,0x22483c00,0x24b63c00,
0x3c00,0x22483c00,0x26483bff,0x28b63bff,0x3c00,0x24b63c00,0x28b63bff,0x2b113bfd,0x3c00,0x26483bff,0x2a483bfe,0x2cb53bfa,
0x3c00,0x27da3bff,0x2bd93bfc,0x2de23bf7,0x3c00,0x28b63bff,0x2cb53bfa,0x2f0e3bf4,0x3c00,0x297f3bfe,0x2d7e3bf8,0x301d3bef,
0x3c00,0x2a483bfe,0x2e463bf6,0x30b23bea,0x3c00,0x2b113bfd,0x2f0e3bf4,0x31473be4,0x3c00,0x2bd93bfc,0x2fd63bf1,0x31db3bdd,
0x3c00,0x2c513bfb,0x304e3bed,0x326f3bd6,0x3c00,0x2cb53bfa,0x30b23bea,0x33033bce,0x3c00,0x2d1a3bf9,0x31153be6,0x33963bc6,
0x3c00,0x2d7e3bf8,0x31793be2,0x34143bbc,0x3c00,0x2de23bf7,0x31db3bdd,0x345d3bb2,0x3c00,0x2e463bf6,0x323e3bd9,0x34a53ba8,
0x3c00,0x2eaa3bf5,0x32a13bd4,0x34ed3b9d,0x3c00,0x2f0e3bf4,0x33033bce,0x35343b91,0x3c00,0x2f723bf2,0x33653bc9,0x357c3b84,
0x3c00,0x2fd63bf1,0x33c63bc3,0x35c23b77,0x3c00,0x301d3bef,0x34143bbc,0x36083b69,0x3c00,0x304e3bed,0x34443bb6,0x364e3b5a,
0x3c00,0x30803bec,0x34753baf,0x36933b4b,0x3c00,0x30b23bea,0x34a53ba8,0x36d73b3b,0x3c00,0x30e43be8,0x34d53ba0,0x371b3b2b,
0x3c00,0x31153be6,0x35053b99,0x375e3b1a,0x3c00,0x31473be4,0x35343b91,0x37a13b08,0x3c00,0x31793be2,0x35643b88,0x37e33af6,
0x3c00,0x31aa3be0,0x35933b80,0x38123ae3,0x3c00,0x31db3bdd,0x35c23b77,0x38323ad0,0x3c00,0x320d3bdb,0x35f13b6e,0x38523abc,
0x3c00,0x3c00,0x3c00,0x3c00,0x26483bff,0x27da3bff,0x28b63bff,0x297f3bfe,0x2a483bfe,0x2bd93bfc,0x2cb53bfa,0x2d7e3bf8,
0x2cb53bfa,0x2de23bf7,0x2f0e3bf4,0x301d3bef,0x2e463bf6,0x2fd63bf1,0x30b23bea,0x31793be2,0x2fd63bf1,0x30e43be8,0x31db3bdd,0x32d23bd1,
0x30b23bea,0x31db3bdd,0x33033bce,0x34143bbc,0x31793be2,0x32d23bd1,0x34143bbc,0x34bd3ba4,0x323e3bd9,0x33c63bc3,0x34a53ba8,0x35643b88,
0x33033bce,0x345d3bb2,0x35343b91,0x36083b69,0x33c63bc3,0x34d53ba0,0x35c23b77,0x36aa3b46,0x34443bb6,0x354c3b8c,0x364e3b5a,0x37483b20,
0x34a53ba8,0x35c23b77,0x36d73b3b,0x37e33af6,0x35053b99,0x36373b5f,0x375e3b1a,0x383d3ac9,0x35643b88,0x36aa3b46,0x37e33af6,0x38873a99,
0x35c23b77,0x371b3b2b,0x38323ad0,0x38ce3a65,0x361f3b64,0x378b3b0e,0x38723aa7,0x39133a2f,0x367c3b50,0x37f93af0,0x38b03a7c,0x395639f6,
0x36d73b3b,0x38323ad0,0x38ec3a4f,0x399639ba,0x37323b25,0x38673aae,0x39273a1f,0x39d4397b,0x378b3b0e,0x389b3a8a,0x395f39ed,0x3a0f393a,
0x37e33af6,0x38ce3a65,0x399639ba,0x3a4738f6,0x381d3add,0x39003a3f,0x39cb3984,0x3a7c38b0,0x38483ac2,0x39303a17,0x39fe394d,0x3aae3867,
0x38723aa7,0x395f39ed,0x3a2f3913,0x3add381d,0x389b3a8a,0x398d39c3,0x3a5e38d8,0x3b0837a1,0x38c43a6d,0x39ba3996,0x3a8a389b,0x3b303705,
0x38ec3a4f,0x39e53969,0x3ab5385d,0x3b553665,0x39133a2f,0x3a0f393a,0x3add381d,0x3b7735c2,0x393a3a0f,0x3a373909,0x3b0237b7,0x3b95351d,
0x395f39ed,0x3a5e38d8,0x3b253732,0x3baf3475,0x398439cb,0x3a8338a6,0x3b4636aa,0x3bc63396,0x3c00,0x3c00,0x3c00,0x3c00,
0x2a483bfe,0x2b113bfd,0x2bd93bfc,0x2c513bfb,0x2e463bf6,0x2f0e3bf4,0x2fd63bf1,0x304e3bed,0x30b23bea,0x31473be4,0x31db3bdd,0x326f3bd6,
0x323e3bd9,0x33033bce,0x33c63bc3,0x34443bb6,0x33c63bc3,0x345d3bb2,0x34d53ba0,0x354c3b8c,0x34a53ba8,0x35343b91,0x35c23b77,0x364e3b5a,
0x35643b88,0x36083b69,0x36aa3b46,0x37483b20,0x361f3b64,0x36d73b3b,0x378b3b0e,0x381d3add,0x36d73b3b,0x37a13b08,0x38323ad0,0x38913a92,
0x378b3b0e,0x38323ad0,0x389b3a8a,0x39003a3f,0x381d3add,0x38913a92,0x39003a3f,0x396939e5,0x38723aa7,0x38ec3a4f,0x395f39ed,0x39cb3984,
0x38c43a6d,0x39433a07,0x39ba3996,0x3a27391d,0x39133a2f,0x399639ba,0x3a0f393a,0x3a7c38b0,0x395f39ed,0x39e53969,0x3a5e38d8,0x3ac9383d,
0x39a839a8,0x3a2f3913,0x3aa73872,0x3b0e378b,0x39ed395f,0x3a7438ba,0x3ae93807,0x3b4b3693,0x3a2f3913,0x3ab5385d,0x3b253732,0x3b803593,
0x3a6d38c4,0x3af037f9,0x3b5a364e,0x3bab348d,0x3aa73872,0x3b253732,0x3b883564,0x3bce3303,0x3add381d,0x3b553665,0x3baf3475,0x3be830e4,
0x3b0e378b,0x3b803593,0x3bce3303,0x3bf82d7e,0x3b3b36d7,0x3ba434bd,0x3be63115,0x3c0024b6,0x3b64361f,0x3bc333c6,0x3bf62e46,0x3bfeaa48,
0x3b883564,0x3bdb320d,0x3bff28b6,0x3bf2af72,0x3ba834a5,0x3bed304e,0x3bffa648,0x3bddb1db,0x3bc333c6,0x3bf92d1a,0x3bf8ad7e,0x3bc0b3f7,
0x3bd9323e,0x3bff2648,0x3beab0b2,0x3b99b505,0x3bea30b2,0x3bffa7da,0x3bd4b2a1,0x3b69b608,0x3bf62e46,0x3bf8ad7e,0x3bb6b444,0x3b30b705,
0x3bfe2a48,0x3becb080,0x3b91b534,0x3af0b7f9,0x3c00,0x3c00,0x3c00,0x3c00,0x2cb53bfa,0x2d1a3bf9,0x2d7e3bf8,0x2de23bf7,
0x30b23bea,0x31153be6,0x31793be2,0x31db3bdd,0x33033bce,0x33963bc6,0x34143bbc,0x345d3bb2,0x34a53ba8,0x35053b99,0x35643b88,0x35c23b77,
0x35c23b77,0x36373b5f,0x36aa3b46,0x371b3b2b,0x36d73b3b,0x375e3b1a,0x37e33af6,0x38323ad0,0x37e33af6,0x383d3ac9,0x38873a99,0x38ce3a65,
0x38723aa7,0x38c43a6d,0x39133a2f,0x395f39ed,0x38ec3a4f,0x39433a07,0x399639ba,0x39e53969,0x395f39ed,0x39ba3996,0x3a0f393a,0x3a5e38d8,
0x39cb3984,0x3a27391d,0x3a7c38b0,0x3ac9383d,0x3a2f3913,0x3a8a389b,0x3add381d,0x3b253732,0x3a8a389b,0x3ae33812,0x3b303705,0x3b7235da,
0x3add381d,0x3b303705,0x3b7735c2,0x3baf3475,0x3b253732,0x3b7235da,0x3baf3475,0x3bdb320d,0x3b64361f,0x3ba834a5,0x3bd9323e,0x3bf62e46,
0x3b993505,0x3bd132d2,0x3bf42f0e,0x3c001e48,0x3bc333c6,0x3bed304e,0x3bff2648,0x3bf8ad7e,0x3be23179,0x3bfd2b11,0x3bfcabd9,0x3be0b1aa,
0x3bf62e46,0x3bffa648,0x3beab0b2,0x3bb6b444,0x3bff2648,0x3bf5aeaa,0x3bc9b365,0x3b7bb5ab,0x3bfeaa48,0x3bddb1db,0x3b99b505,0x3b30b705,
0x3bf1afd6,0x3bb9b42c,0x3b5ab64e,0x3ad6b828,0x3bd9b23e,0x3b88b564,0x3b0eb78b,0x3a6db8c4,0x3bb6b444,0x3b4bb693,0x3ab5b85d,0x39f6b956,
0x3b88b564,0x3b02b7b7,0x3a4fb8ec,0x3972b9dc,0x3b50b67c,0x3aaeb867,0x39dcb972,0x38e2ba56,0x3b0eb78b,0x3a4fb8ec,0x395fb9ed,0x3848bac2,
0x3ac2b848,0x39e5b969,0x38d8ba5e,0x3748bb20,0x3a6db8c4,0x3972b9dc,0x3848bac2,0x35f1bb6e,0x3a0fb93a,0x38f6ba47,0x375ebb1a,0x348dbbab,
0x3c00,0x3c00,0x3c00,0x3c00,0x2e463bf6,0x2eaa3bf5,0x2f0e3bf4,0x2f723bf2,0x323e3bd9,0x32a13bd4,0x33033bce,0x33653bc9,
0x34a53ba8,0x34ed3b9d,0x35343b91,0x357c3b84,0x361f3b64,0x367c3b50,0x36d73b3b,0x37323b25,0x378b3b0e,0x37f93af0,0x38323ad0,0x38673aae,
0x38723aa7,0x38b03a7c,0x38ec3a4f,0x39273a1f,0x39133a2f,0x395639f6,0x399639ba,0x39d4397b,0x39a839a8,0x39ed395f,0x3a2f3913,0x3a6d38c4,
0x3a2f3913,0x3a7438ba,0x3ab5385d,0x3af037f9,0x3aa73872,0x3ae93807,0x3b253732,0x3b5a364e,0x3b0e378b,0x3b4b3693,0x3b803593,0x3bab348d,
0x3b64361f,0x3b993505,0x3bc333c6,0x3be23179,0x3ba834a5,0x3bd132d2,0x3bed304e,0x3bfd2b11,0x3bd9323e,0x3bf42f0e,0x3bff2648,0x3bfcabd9,
0x3bf62e46,0x3c001e48,0x3bf8ad7e,0x3be0b1aa,0x3c000000,0x3bf6ae46,0x3bd9b23e,0x3ba8b4a5,0x3bf6ae46,0x3bd6b26f,0x3ba0b4d5,0x3b55b665,
0x3bd9b23e,0x3ba0b4d5,0x3b50b67c,0x3ae9b807,0x3ba8b4a5,0x3b55b665,0x3ae9b807,0x3a65b8ce,0x3b64b61f,0x3af6b7e3,0x3a6db8c4,0x39cbb984,
0x3b0eb78b,0x3a83b8a6,0x39dcb972,0x391dba27,0x3aa7b872,0x39feb94d,0x393aba0f,0x385dbab5,0x3a2fb913,0x3969b9e5,0x3887ba99,0x371bbb2b,
0x39a8b9a8,0x38c4ba6d,0x378bbb0e,0x3564bb88,0x3913ba2f,0x3812bae3,0x35f1bb6e,0x3334bbcb,0x3872baa7,0x36aabb46,0x3444bbb6,0x2f0ebbf4,
0x378bbb0e,0x351dbb95,0x3115bbe6,0x9e48bc00,0x361fbb64,0x3303bbce,0x2a48bbfe,0xafd6bbf1,0x34a5bba8,0x2f72bbf2,0xabd9bbfc,0xb396bbc6,
0x323ebbd9,0x2248bc00,0xb179bbe2,0xb593bb80,0x2e46bbf6,0xade2bbf7,0xb475bbaf,0xb748bb20,0x3c00,0x3c00,0x3c00,0x3c00,
0x2fd63bf1,0x301d3bef,0x304e3bed,0x30803bec,0x33c63bc3,0x34143bbc,0x34443bb6,0x34753baf,0x35c23b77,0x36083b69,0x364e3b5a,0x36933b4b,
0x378b3b0e,0x37e33af6,0x381d3add,0x38483ac2,0x389b3a8a,0x38ce3a65,0x39003a3f,0x39303a17,0x395f39ed,0x399639ba,0x39cb3984,0x39fe394d,
0x3a0f393a,0x3a4738f6,0x3a7c38b0,0x3aae3867,0x3aa73872,0x3add381d,0x3b0e378b,0x3b3b36d7,0x3b253732,0x3b553665,0x3b803593,0x3ba434bd,
0x3b883564,0x3baf3475,0x3bce3303,0x3be63115,0x3bce3303,0x3be830e4,0x3bf82d7e,0x3c0024b6,0x3bf62e46,0x3bff2648,0x3bfeaa48,0x3bf1afd6,
0x3bffa648,0x3bf5aeaa,0x3bddb1db,0x3bb9b42c,0x3beab0b2,0x3bc9b365,0x3b99b505,0x3b5ab64e,0x3bb6b444,0x3b7bb5ab,0x3b30b705,0x3ad6b828,
0x3b64b61f,0x3b0eb78b,0x3aa7b872,0x3a2fb913,0x3af6b7e3,0x3a83b8a6,0x39feb94d,0x3969b9e5,0x3a6db8c4,0x39dcb972,0x393aba0f,0x3887ba99,
0x39cbb984,0x391dba27,0x385dbab5,0x371bbb2b,0x3913ba2f,0x3848bac2,0x36d7bb3b,0x3505bb99,0x3848bac2,0x36c1bb41,0x34d5bba0,0x31aabbe0,
0x36d7bb3b,0x34d5bba0,0x3179bbe2,0x28b6bbff,0x3505bb99,0x31aabbe0,0x28b6bbff,0xaeaabbf5,0x323ebbd9,0x2a48bbfe,0xae46bbf6,0xb3c6bbc3,
0x2cb5bbfa,0xad1abbf9,0xb365bbc9,0xb608bb69,0xaa48bbfe,0xb2a1bbd4,0xb5c2bb77,0xb807bae9,0xb179bbe2,0xb54cbb8c,0xb7b7bb02,0xb8f6ba47,
0xb4a5bba8,0xb732bb25,0xb8c4ba6d,0xb9cbb984,0xb67cbb50,0xb87cbaa0,0xb996b9ba,0xba83b8a6,0xb81dbadd,0xb94db9fe,0xba4fb8ec,0xbb1ab75e,
0xb8ecba4f,0xba07b943,0xbae9b807,0xbb8cb54c,0x3c00,0x3c00,0x3c00,0x3c00,0x30b23bea,0x30e43be8,0x31153be6,0x31473be4,
0x34a53ba8,0x34d53ba0,0x35053b99,0x35343b91,0x36d73b3b,0x371b3b2b,0x375e3b1a,0x37a13b08,0x38723aa7,0x389b3a8a,0x38c43a6d,0x38ec3a4f,
0x395f39ed,0x398d39c3,0x39ba3996,0x39e53969,0x3a2f3913,0x3a5e38d8,0x3a8a389b,0x3ab5385d,0x3add381d,0x3b0837a1,0x3b303705,0x3b553665,
0x3b64361f,0x3b883564,0x3ba834a5,0x3bc333c6,0x3bc333c6,0x3bdb320d,0x3bed304e,0x3bf92d1a,0x3bf62e46,0x3bff28b6,0x3bffa648,0x3bf8ad7e,
0x3bfeaa48,0x3bf2af72,0x3bddb1db,0x3bc0b3f7,0x3bd9b23e,0x3bb6b444,0x3b88b564,0x3b50b67c,0x3b88b564,0x3b4bb693,0x3b02b7b7,0x3aaeb867,
0x3b0eb78b,0x3ab5b85d,0x3a4fb8ec,0x39dcb972,0x3a6db8c4,0x39f6b956,0x3972b9dc,0x38e2ba56,0x39a8b9a8,0x3913ba2f,0x3872baa7,0x378bbb0e,
0x38c4ba6d,0x3812bae3,0x36aabb46,0x351dbb95,0x378bbb0e,0x35f1bb6e,0x3444bbb6,0x3115bbe6,0x3564bb88,0x3334bbcb,0x2f0ebbf4,0x9e48bc00,
0x323ebbd9,0x2cb5bbfa,0xaa48bbfe,0xb179bbe2,0x2a48bbfe,0xad1abbf9,0xb2a1bbd4,0xb54cbb8c,0xae46bbf6,0xb365bbc9,0xb5c2bb77,0xb7b7bb02,
0xb3c6bbc3,0xb608bb69,0xb807bae9,0xb8f6ba47,0xb61fbb64,0xb81dbadd,0xb913ba2f,0xb9edb95f,0xb81dbadd,0xb91dba27,0xb9feb94d,0xbabcb852,
0xb913ba2f,0xb9feb94d,0xbac2b848,0xbb5ab64e,0xb9edb95f,0xbabcb852,0xbb5ab64e,0xbbc6b396,0xbaa7b872,0xbb50b67c,0xbbc3b3c6,0xbbfaacb5,
0xbb3bb6d7,0xbbb9b42c,0xbbf8ad7e,0xbbf72de2,0xbba8b4a5,0xbbf4af0e,0xbbfa2cb5,0xbbbc3414,0xbbeab0b2,0xbbfe297f,0xbbc93365,0xbb4b3693,
0x3c00,0x3c00,0x3c00,0x3c00,0x31793be2,0x31aa3be0,0x31db3bdd,0x320d3bdb,0x35643b88,0x35933b80,0x35c23b77,0x35f13b6e,
0x37e33af6,0x38123ae3,0x38323ad0,0x38523abc,0x39133a2f,0x393a3a0f,0x395f39ed,0x398439cb,0x3a0f393a,0x3a373909,0x3a5e38d8,0x3a8338a6,
0x3add381d,0x3b0237b7,0x3b253732,0x3b4636aa,0x3b7735c2,0x3b95351d,0x3baf3475,0x3bc63396,0x3bd9323e,0x3bea30b2,0x3bf62e46,0x3bfe2a48,
0x3bff2648,0x3bffa7da,0x3bf8ad7e,0x3becb080,0x3beab0b2,0x3bd4b2a1,0x3bb6b444,0x3b91b534,0x3b99b505,0x3b69b608,0x3b30b705,0x3af0b7f9,
0x3b0eb78b,0x3ac2b848,0x3a6db8c4,0x3a0fb93a,0x3a4fb8ec,0x39e5b969,0x3972b9dc,0x38f6ba47,0x395fb9ed,0x38d8ba5e,0x3848bac2,0x375ebb1a,
0x3848bac2,0x3748bb20,0x35f1bb6e,0x348dbbab,0x361fbb64,0x34a5bba8,0x323ebbd9,0x2e46bbf6,0x3303bbce,0x2f72bbf2,0x2248bc00,0xade2bbf7,
0x2a48bbfe,0xabd9bbfc,0xb179bbe2,0xb475bbaf,0xafd6bbf1,0xb396bbc6,0xb593bb80,0xb748bb20,0xb4a5bba8,0xb67cbb50,0xb81dbadd,0xb8ecba4f,
0xb732bb25,0xb87cbaa0,0xb94db9fe,0xba07b943,0xb8c4ba6d,0xb996b9ba,0xba4fb8ec,0xbae9b807,0xb9cbb984,0xba83b8a6,0xbb1ab75e,0xbb8cb54c,
0xbaa7b872,0xbb3bb6d7,0xbba8b4a5,0xbbeab0b2,0xbb50b67c,0xbbb9b42c,0xbbf4af0e,0xbbfe297f,0xbbc3b3c6,0xbbf8ad7e,0xbbfa2cb5,0xbbc93365,
0xbbfaacb5,0xbbf72de2,0xbbbc3414,0xbb4b3693,0xbbf62e46,0xbbb63444,0xbb3b36d7,0xba8a389b,0xbbb63444,0xbb3636ee,0xba7c38b0,0xb98d39c3,
0xbb3b36d7,0xba7c38b0,0xb98439cb,0xb85d3ab5,0xba8a389b,0xb98d39c3,0xb85d3ab5,0xb6083b69,0x3c00,0x2e463bf6,0x323e3bd9,0x34a53ba8,
0x1a483c00,0x2e783bf6,0x32573bd7,0x34b13ba6,0x1e483c00,0x2eaa3bf5,0x326f3bd6,0x34bd3ba4,0x20b63c00,0x2edc3bf4,0x32883bd5,0x34c93ba2,
0x22483c00,0x2f0e3bf4,0x32a13bd4,0x34d53ba0,0x23db3c00,0x2f403bf3,0x32b93bd2,0x34e13b9e,0x24b63c00,0x2f723bf2,0x32d23bd1,0x34ed3b9d,
0x257f3c00,0x2fa43bf1,0x32ea3bd0,0x34f93b9b,0x26483bff,0x2fd63bf1,0x33033bce,0x35053b99,0x27113bff,0x30043bf0,0x331b3bcd,0x35113b97,
0x27da3bff,0x301d3bef,0x33343bcb,0x351d3b95,0x28523bff,0x30363bee,0x334c3bca,0x35293b93,0x28b63bff,0x304e3bed,0x33653bc9,0x35343b91,
0x291b3bfe,0x30673bed,0x337d3bc7,0x35403b8f,0x297f3bfe,0x30803bec,0x33963bc6,0x354c3b8c,0x29e33bfe,0x30993beb,0x33ae3bc4,0x35583b8a,
0x2a483bfe,0x30b23bea,0x33c63bc3,0x35643b88,0x2aac3bfd,0x30cb3be9,0x33df3bc1,0x35703b86,0x2b113bfd,0x30e43be8,0x33f73bc0,0x357c3b84,
0x2b753bfd,0x30fd3be7,0x34083bbe,0x35873b82,0x2bd93bfc,0x31153be6,0x34143bbc,0x35933b80,0x2c1f3bfc,0x312e3be5,0x34203bbb,0x359f3b7d,
0x2c513bfb,0x31473be4,0x342c3bb9,0x35ab3b7b,0x2c833bfb,0x31603be3,0x34383bb7,0x35b63b79,0x2cb53bfa,0x31793be2,0x34443bb6,0x35c23b77,
0x2ce73bfa,0x31913be1,0x34513bb4,0x35ce3b74,0x2d1a3bf9,0x31aa3be0,0x345d3bb2,0x35da3b72,0x2d4c3bf9,0x31c33bdf,0x34693bb1,0x35e53b70,
0x2d7e3bf8,0x31db3bdd,0x34753baf,0x35f13b6e,0x2db03bf8,0x31f43bdc,0x34813bad,0x35fd3b6b,0x2de23bf7,0x320d3bdb,0x348d3bab,0x36083b69,
0x2e143bf7,0x32263bda,0x34993baa,0x36143b67,0x361f3b64,0x378b3b0e,0x38723aa7,0x39133a2f,0x362b3b62,0x37963b0b,0x38773aa3,0x39183a2b,
0x36373b5f,0x37a13b08,0x387c3aa0,0x391d3a27,0x36423b5d,0x37ac3b05,0x38813a9c,0x39223a23,0x364e3b5a,0x37b73b02,0x38873a99,0x39273a1f,
0x36593b58,0x37c23aff,0x388c3a95,0x392b3a1b,0x36653b55,0x37cd3afc,0x38913a92,0x39303a17,0x36703b53,0x37d83af9,0x38963a8e,0x39353a13,
0x367c3b50,0x37e33af6,0x389b3a8a,0x393a3a0f,0x36873b4e,0x37ee3af3,0x38a03a87,0x393e3a0b,0x36933b4b,0x37f93af0,0x38a63a83,0x39433a07,
0x369e3b49,0x38023aed,0x38ab3a7f,0x39483a02,0x36aa3b46,0x38073ae9,0x38b03a7c,0x394d39fe,0x36b53b43,0x380d3ae6,0x38b53a78,0x395139fa,
0x36c13b41,0x38123ae3,0x38ba3a74,0x395639f6,0x36cc3b3e,0x38173ae0,0x38bf3a71,0x395b39f2,0x36d73b3b,0x381d3add,0x38c43a6d,0x395f39ed,
0x36e33b39,0x38223ad9,0x38c93a69,0x396439e9,0x36ee3b36,0x38283ad6,0x38ce3a65,0x396939e5,0x36f93b33,0x382d3ad3,0x38d33a62,0x396d39e1,
0x37053b30,0x38323ad0,0x38d83a5e,0x397239dc,0x37103b2e,0x38383acc,0x38dd3a5a,0x397639d8,0x371b3b2b,0x383d3ac9,0x38e23a56,0x397b39d4,
0x37263b28,0x38423ac6,0x38e73a52,0x398039d0,0x37323b25,0x38483ac2,0x38ec3a4f,0x398439cb,0x373d3b22,0x384d3abf,0x38f13a4b,0x398939c7,
0x37483b20,0x38523abc,0x38f63a47,0x398d39c3,0x37533b1d,0x38583ab8,0x38fb3a43,0x399239be,0x375e3b1a,0x385d3ab5,0x39003a3f,0x399639ba,
0x376a3b17,0x38623ab1,0x39053a3b,0x399b39b5,0x37753b14,0x38673aae,0x39093a37,0x399f39b1,0x37803b11,0x386d3aaa,0x390e3a33,0x39a439ad,
0x39a839a8,0x3a2f3913,0x3aa73872,0x3b0e378b,0x39ad39a4,0x3a33390e,0x3aaa386d,0x3b113780,0x39b1399f,0x3a373909,0x3aae3867,0x3b143775,
0x39b5399b,0x3a3b3905,0x3ab13862,0x3b17376a,0x39ba3996,0x3a3f3900,0x3ab5385d,0x3b1a375e,0x39be3992,0x3a4338fb,0x3ab83858,0x3b1d3753,
0x39c3398d,0x3a4738f6,0x3abc3852,0x3b203748,0x39c73989,0x3a4b38f1,0x3abf384d,0x3b22373d,0x39cb3984,0x3a4f38ec,0x3ac23848,0x3b253732,
0x39d03980,0x3a5238e7,0x3ac63842,0x3b283726,0x39d4397b,0x3a5638e2,0x3ac9383d,0x3b2b371b,0x39d83976,0x3a5a38dd,0x3acc3838,0x3b2e3710,
0x39dc3972,0x3a5e38d8,0x3ad03832,0x3b303705,0x39e1396d,0x3a6238d3,0x3ad3382d,0x3b3336f9,0x39e53969,0x3a6538ce,0x3ad63828,0x3b3636ee,
0x39e93964,0x3a6938c9,0x3ad93822,0x3b3936e3,0x39ed395f,0x3a6d38c4,0x3add381d,0x3b3b36d7,0x39f2395b,0x3a7138bf,0x3ae03817,0x3b3e36cc,
0x39f63956,0x3a7438ba,0x3ae33812,0x3b4136c1,0x39fa3951,0x3a7838b5,0x3ae6380d,0x3b4336b5,0x39fe394d,0x3a7c38b0,0x3ae93807,0x3b4636aa,
0x3a023948,0x3a7f38ab,0x3aed3802,0x3b49369e,0x3a073943,0x3a8338a6,0x3af037f9,0x3b4b3693,0x3a0b393e,0x3a8738a0,0x3af337ee,0x3b4e3687,
0x3a0f393a,0x3a8a389b,0x3af637e3,0x3b50367c,0x3a133935,0x3a8e3896,0x3af937d8,0x3b533670,0x3a173930,0x3a923891,0x3afc37cd,0x3b553665,
0x3a1b392b,0x3a95388c,0x3aff37c2,0x3b583659,0x3a1f3927,0x3a993887,0x3b0237b7,0x3b5a364e,0x3a233922,0x3a9c3881,0x3b0537ac,0x3b5d3642,
0x3a27391d,0x3aa0387c,0x3b0837a1,0x3b5f3637,0x3a2b3918,0x3aa33877,0x3b0b3796,0x3b62362b,0x3b64361f,0x3ba834a5,0x3bd9323e,0x3bf62e46,
0x3b673614,0x3baa3499,0x3bda3226,0x3bf72e14,0x3b693608,0x3bab348d,0x3bdb320d,0x3bf72de2,0x3b6b35fd,0x3bad3481,0x3bdc31f4,0x3bf82db0,
0x3b6e35f1,0x3baf3475,0x3bdd31db,0x3bf82d7e,0x3b7035e5,0x3bb13469,0x3bdf31c3,0x3bf92d4c,0x3b7235da,0x3bb2345d,0x3be031aa,0x3bf92d1a,
0x3b7435ce,0x3bb43451,0x3be13191,0x3bfa2ce7,0x3b7735c2,0x3bb63444,0x3be23179,0x3bfa2cb5,0x3b7935b6,0x3bb73438,0x3be33160,0x3bfb2c83,
0x3b7b35ab,0x3bb9342c,0x3be43147,0x3bfb2c51,0x3b7d359f,0x3bbb3420,0x3be5312e,0x3bfc2c1f,0x3b803593,0x3bbc3414,0x3be63115,0x3bfc2bd9,
0x3b823587,0x3bbe3408,0x3be730fd,0x3bfd2b75,0x3b84357c,0x3bc033f7,0x3be830e4,0x3bfd2b11,0x3b863570,0x3bc133df,0x3be930cb,0x3bfd2aac,
0x3b883564,0x3bc333c6,0x3bea30b2,0x3bfe2a48,0x3b8a3558,0x3bc433ae,0x3beb3099,0x3bfe29e3,0x3b8c354c,0x3bc63396,0x3bec3080,0x3bfe297f,
0x3b8f3540,0x3bc7337d,0x3bed3067,0x3bfe291b,0x3b913534,0x3bc93365,0x3bed304e,0x3bff28b6,0x3b933529,0x3bca334c,0x3bee3036,0x3bff2852,
0x3b95351d,0x3bcb3334,0x3bef301d,0x3bff27da,0x3b973511,0x3bcd331b,0x3bf03004,0x3bff2711,0x3b993505,0x3bce3303,0x3bf12fd6,0x3bff2648,
0x3b9b34f9,0x3bd032ea,0x3bf12fa4,0x3c00257f,0x3b9d34ed,0x3bd132d2,0x3bf22f72,0x3c0024b6,0x3b9e34e1,0x3bd232b9,0x3bf32f40,0x3c0023db,
0x3ba034d5,0x3bd432a1,0x3bf42f0e,0x3c002248,0x3ba234c9,0x3bd53288,0x3bf42edc,0x3c0020b6,0x3ba434bd,0x3bd6326f,0x3bf52eaa,0x3c001e48,
0x3ba634b1,0x3bd73257,0x3bf62e78,0x3c001a48,0x3c000000,0x3bf6ae46,0x3bd9b23e,0x3ba8b4a5,0x3c009a48,0x3bf6ae78,0x3bd7b257,0x3ba6b4b1,
0x3c009e48,0x3bf5aeaa,0x3bd6b26f,0x3ba4b4bd,0x3c00a0b6,0x3bf4aedc,0x3bd5b288,0x3ba2b4c9,0x3c00a248,0x3bf4af0e,0x3bd4b2a1,0x3ba0b4d5,
0x3c00a3db,0x3bf3af40,0x3bd2b2b9,0x3b9eb4e1,0x3c00a4b6,0x3bf2af72,0x3bd1b2d2,0x3b9db4ed,0x3c00a57f,0x3bf1afa4,0x3bd0b2ea,0x3b9bb4f9,
0x3bffa648,0x3bf1afd6,0x3bceb303,0x3b99b505,0x3bffa711,0x3bf0b004,0x3bcdb31b,0x3b97b511,0x3bffa7da,0x3befb01d,0x3bcbb334,0x3b95b51d,
0x3bffa852,0x3beeb036,0x3bcab34c,0x3b93b529,0x3bffa8b6,0x3bedb04e,0x3bc9b365,0x3b91b534,0x3bfea91b,0x3bedb067,0x3bc7b37d,0x3b8fb540,
0x3bfea97f,0x3becb080,0x3bc6b396,0x3b8cb54c,0x3bfea9e3,0x3bebb099,0x3bc4b3ae,0x3b8ab558,0x3bfeaa48,0x3beab0b2,0x3bc3b3c6,0x3b88b564,
0x3bfdaaac,0x3be9b0cb,0x3bc1b3df,0x3b86b570,0x3bfdab11,0x3be8b0e4,0x3bc0b3f7,0x3b84b57c,0x3bfdab75,0x3be7b0fd,0x3bbeb408,0x3b82b587,
0x3bfcabd9,0x3be6b115,0x3bbcb414,0x3b80b593,0x3bfcac1f,0x3be5b12e,0x3bbbb420,0x3b7db59f,0x3bfbac51,0x3be4b147,0x3bb9b42c,0x3b7bb5ab,
0x3bfbac83,0x3be3b160,0x3bb7b438,0x3b79b5b6,0x3bfaacb5,0x3be2b179,0x3bb6b444,0x3b77b5c2,0x3bfaace7,0x3be1b191,0x3bb4b451,0x3b74b5ce,
0x3bf9ad1a,0x3be0b1aa,0x3bb2b45d,0x3b72b5da,0x3bf9ad4c,0x3bdfb1c3,0x3bb1b469,0x3b70b5e5,0x3bf8ad7e,0x3bddb1db,0x3bafb475,0x3b6eb5f1,
0x3bf8adb0,0x3bdcb1f4,0x3badb481,0x3b6bb5fd,0x3bf7ade2,0x3bdbb20d,0x3babb48d,0x3b69b608,0x3bf7ae14,0x3bdab226,0x3baab499,0x3b67b614,
0x3b64b61f,0x3b0eb78b,0x3aa7b872,0x3a2fb913,0x3b62b62b,0x3b0bb796,0x3aa3b877,0x3a2bb918,0x3b5fb637,0x3b08b7a1,0x3aa0b87c,0x3a27b91d,
0x3b5db642,0x3b05b7ac,0x3a9cb881,0x3a23b922,0x3b5ab64e,0x3b02b7b7,0x3a99b887,0x3a1fb927,0x3b58b659,0x3affb7c2,0x3a95b88c,0x3a1bb92b,
0x3b55b665,0x3afcb7cd,0x3a92b891,0x3a17b930,0x3b53b670,0x3af9b7d8,0x3a8eb896,0x3a13b935,0x3b50b67c,0x3af6b7e3,0x3a8ab89b,0x3a0fb93a,
0x3b4eb687,0x3af3b7ee,0x3a87b8a0,0x3a0bb93e,0x3b4bb693,0x3af0b7f9,0x3a83b8a6,0x3a07b943,0x3b49b69e,0x3aedb802,0x3a7fb8ab,0x3a02b948,
0x3b46b6aa,0x3ae9b807,0x3a7cb8b0,0x39feb94d,0x3b43b6b5,0x3ae6b80d,0x3a78b8b5,0x39fab951,0x3b41b6c1,0x3ae3b812,0x3a74b8ba,0x39f6b956,
0x3b3eb6cc,0x3ae0b817,0x3a71b8bf,0x39f2b95b,0x3b3bb6d7,0x3addb81d,0x3a6db8c4,0x39edb95f,0x3b39b6e3,0x3ad9b822,0x3a69b8c9,0x39e9b964,
0x3b36b6ee,0x3ad6b828,0x3a65b8ce,0x39e5b969,0x3b33b6f9,0x3ad3b82d,0x3a62b8d3,0x39e1b96d,0x3b30b705,0x3ad0b832,0x3a5eb8d8,0x39dcb972,
0x3b2eb710,0x3accb838,0x3a5ab8dd,0x39d8b976,0x3b2bb71b,0x3ac9b83d,0x3a56b8e2,0x39d4b97b,0x3b28b726,0x3ac6b842,0x3a52b8e7,0x39d0b980,
0x3b25b732,0x3ac2b848,0x3a4fb8ec,0x39cbb984,0x3b22b73d,0x3abfb84d,0x3a4bb8f1,0x39c7b989,0x3b20b748,0x3abcb852,0x3a47b8f6,0x39c3b98d,
0x3b1db753,0x3ab8b858,0x3a43b8fb,0x39beb992,0x3b1ab75e,0x3ab5b85d,0x3a3fb900,0x39bab996,0x3b17b76a,0x3ab1b862,0x3a3bb905,0x39b5b99b,
0x3b14b775,0x3aaeb867,0x3a37b909,0x39b1b99f,0x3b11b780,0x3aaab86d,0x3a33b90e,0x39adb9a4,0x39a8b9a8,0x3913ba2f,0x3872baa7,0x378bbb0e,
0x39a4b9ad,0x390eba33,0x386dbaaa,0x3780bb11,0x399fb9b1,0x3909ba37,0x3867baae,0x3775bb14,0x399bb9b5,0x3905ba3b,0x3862bab1,0x376abb17,
0x3996b9ba,0x3900ba3f,0x385dbab5,0x375ebb1a,0x3992b9be,0x38fbba43,0x3858bab8,0x3753bb1d,0x398db9c3,0x38f6ba47,0x3852babc,0x3748bb20,
0x3989b9c7,0x38f1ba4b,0x384dbabf,0x373dbb22,0x3984b9cb,0x38ecba4f,0x3848bac2,0x3732bb25,0x3980b9d0,0x38e7ba52,0x3842bac6,0x3726bb28,
0x397bb9d4,0x38e2ba56,0x383dbac9,0x371bbb2b,0x3976b9d8,0x38ddba5a,0x3838bacc,0x3710bb2e,0x3972b9dc,0x38d8ba5e,0x3832bad0,0x3705bb30,
0x396db9e1,0x38d3ba62,0x382dbad3,0x36f9bb33,0x3969b9e5,0x38ceba65,0x3828bad6,0x36eebb36,0x3964b9e9,0x38c9ba69,0x3822bad9,0x36e3bb39,
0x395fb9ed,0x38c4ba6d,0x381dbadd,0x36d7bb3b,0x395bb9f2,0x38bfba71,0x3817bae0,0x36ccbb3e,0x3956b9f6,0x38baba74,0x3812bae3,0x36c1bb41,
0x3951b9fa,0x38b5ba78,0x380dbae6,0x36b5bb43,0x394db9fe,0x38b0ba7c,0x3807bae9,0x36aabb46,0x3948ba02,0x38abba7f,0x3802baed,0x369ebb49,
0x3943ba07,0x38a6ba83,0x37f9baf0,0x3693bb4b,0x393eba0b,0x38a0ba87,0x37eebaf3,0x3687bb4e,0x393aba0f,0x389bba8a,0x37e3baf6,0x367cbb50,
0x3935ba13,0x3896ba8e,0x37d8baf9,0x3670bb53,0x3930ba17,0x3891ba92,0x37cdbafc,0x3665bb55,0x392bba1b,0x388cba95,0x37c2baff,0x3659bb58,
0x3927ba1f,0x3887ba99,0x37b7bb02,0x364ebb5a,0x3922ba23,0x3881ba9c,0x37acbb05,0x3642bb5d,0x391dba27,0x387cbaa0,0x37a1bb08,0x3637bb5f,
0x3918ba2b,0x3877baa3,0x3796bb0b,0x362bbb62,0x361fbb64,0x34a5bba8,0x323ebbd9,0x2e46bbf6,0x3614bb67,0x3499bbaa,0x3226bbda,0x2e14bbf7,
0x3608bb69,0x348dbbab,0x320dbbdb,0x2de2bbf7,0x35fdbb6b,0x3481bbad,0x31f4bbdc,0x2db0bbf8,0x35f1bb6e,0x3475bbaf,0x31dbbbdd,0x2d7ebbf8,
0x35e5bb70,0x3469bbb1,0x31c3bbdf,0x2d4cbbf9,0x35dabb72,0x345dbbb2,0x31aabbe0,0x2d1abbf9,0x35cebb74,0x3451bbb4,0x3191bbe1,0x2ce7bbfa,
0x35c2bb77,0x3444bbb6,0x3179bbe2,0x2cb5bbfa,0x35b6bb79,0x3438bbb7,0x3160bbe3,0x2c83bbfb,0x35abbb7b,0x342cbbb9,0x3147bbe4,0x2c51bbfb,
0x359fbb7d,0x3420bbbb,0x312ebbe5,0x2c1fbbfc,0x3593bb80,0x3414bbbc,0x3115bbe6,0x2bd9bbfc,0x3587bb82,0x3408bbbe,0x30fdbbe7,0x2b75bbfd,
0x357cbb84,0x33f7bbc0,0x30e4bbe8,0x2b11bbfd,0x3570bb86,0x33dfbbc1,0x30cbbbe9,0x2aacbbfd,0x3564bb88,0x33c6bbc3,0x30b2bbea,0x2a48bbfe,
0x3558bb8a,0x33aebbc4,0x3099bbeb,0x29e3bbfe,0x354cbb8c,0x3396bbc6,0x3080bbec,0x297fbbfe,0x3540bb8f,0x337dbbc7,0x3067bbed,0x291bbbfe,
0x3534bb91,0x3365bbc9,0x304ebbed,0x28b6bbff,0x3529bb93,0x334cbbca,0x3036bbee,0x2852bbff,0x351dbb95,0x3334bbcb,0x301dbbef,0x27dabbff,
0x3511bb97,0x331bbbcd,0x3004bbf0,0x2711bbff,0x3505bb99,0x3303bbce,0x2fd6bbf1,0x2648bbff,0x34f9bb9b,0x32eabbd0,0x2fa4bbf1,0x257fbc00,
0x34edbb9d,0x32d2bbd1,0x2f72bbf2,0x24b6bc00,0x34e1bb9e,0x32b9bbd2,0x2f40bbf3,0x23dbbc00,0x34d5bba0,0x32a1bbd4,0x2f0ebbf4,0x2248bc00,
0x34c9bba2,0x3288bbd5,0x2edcbbf4,0x20b6bc00,0x34bdbba4,0x326fbbd6,0x2eaabbf5,0x1e48bc00,0x34b1bba6,0x3257bbd7,0x2e78bbf6,0x1a48bc00,
};

#define TROW 648
#define WS_E     0
#define WS_GM    0x1000
#define WS_GW    0x5000
#define WS_GD    0x5800
#define WS_GF    0x6000
#define WS_T1    0x10000
#define WS_T2    WS_T1
#define WS_H2    (WS_T2 + 256*TROW*16)
#define WS_X1    (WS_H2 + 64*40*1024)
#define WPS 8320
#define WS_WP    (WS_X1 + 1024*640*4)
#define WS_WT1   (WS_WP + 3989504)
#define WS_WT2   (WS_WT1 + 96*40*1024)
#define WS_HT    (WS_WT2 + 8*240*1024)
#define WS_END   (WS_HT + 128*48*1024)
#define SCR 122880
#define SPINMAX 20000
#define SENT 0xFFFFFFFFu

DI float bflo(u32 v){ return __uint_as_float(v<<16); }
DI float bfhi(u32 v){ return __uint_as_float(v & 0xffff0000u); }
DI u32 pkbf(float a, float b){ bf16x2 q = {(__bf16)a, (__bf16)b}; u32 r = __builtin_bit_cast(u32, q); asm("" : "+v"(r)); return r; }
DI float relu(float a){ float r; asm("v_max_f32 %0,0,%1" : "=v"(r) : "v"(a)); return r; }
DI float rbf(float a){ return (float)(__bf16)a; }
DI u32 pkh(float a, float b){ f16x2 r = {(_Float16)a,(_Float16)b}; return __builtin_bit_cast(u32,r); }
DI f16x2 ash(u32 v){ return __builtin_bit_cast(f16x2,v); }
DI u32 asu(f16x2 v){ return __builtin_bit_cast(u32,v); }
DI u32 cmul(u32 x, u32 w){ u32 r;
  asm("v_pk_mul_f16 %0,%1,%2 op_sel_hi:[1,0]\n v_pk_fma_f16 %0,%1,%2,%0 op_sel:[1,1,0] op_sel_hi:[0,1,1] neg_hi:[1,0,0]" : "=&v"(r) : "v"(x), "v"(w)); return r; }
DI u32 cmul2(u32 x, u32 w, u32 y, u32 z){ u32 r;
  asm("v_pk_mul_f16 %0,%1,%2 op_sel_hi:[1,0]\n v_pk_fma_f16 %0,%1,%2,%0 op_sel:[1,1,0] op_sel_hi:[0,1,1] neg_hi:[1,0,0]\n"
      "v_pk_fma_f16 %0,%3,%4,%0 op_sel_hi:[1,0,1]\n v_pk_fma_f16 %0,%3,%4,%0 op_sel:[1,1,0] op_sel_hi:[0,1,1] neg_hi:[1,0,0]" : "=&v"(r) : "v"(x), "v"(w), "v"(y), "v"(z)); return r; }
DI u32 rot90(u32 p){ u32 r; asm("v_pk_mul_f16 %0,%1,1.0 op_sel:[1,0] op_sel_hi:[0,0] neg_lo:[1,0]" : "=v"(r) : "v"(p)); return r; }
DI u32x4 rot90x4(u32x4 p){ u32x4 r; r.x=rot90(p.x); r.y=rot90(p.y); r.z=rot90(p.z); r.w=rot90(p.w); return r; }
DI void mf2z(f32x16& d0, f32x16& d1, u32x4 a0, u32x4 b0, u32x4 a1, u32x4 b1){
  asm volatile("s_nop 1\n v_mfma_f32_32x32x16_f16 %0,%2,%3,0\n v_mfma_f32_32x32x16_f16 %1,%4,%5,0" : "=&v"(d0), "=&v"(d1) : "v"(a0), "v"(b0), "v"(a1), "v"(b1)); }
DI void mf2(f32x16& d0, f32x16& d1, u32x4 a0, u32x4 b0, u32x4 a1, u32x4 b1){
  asm volatile("s_nop 1\n v_mfma_f32_32x32x16_f16 %0,%2,%3,%0\n v_mfma_f32_32x32x16_f16 %1,%4,%5,%1" : "+v"(d0), "+v"(d1) : "v"(a0), "v"(b0), "v"(a1), "v"(b1)); }
DI void mfdone(f32x16& d0, f32x16& d1){ asm volatile("s_nop 11" : "+v"(d0), "+v"(d1)); }
DI void mfdone9(f32x16& d0, f32x16& d1){ asm volatile("s_nop 9" : "+v"(d0), "+v"(d1)); }

template<int CTRL> DI float dpp(float v){ return __builtin_bit_cast(float, __builtin_amdgcn_update_dpp(0, __builtin_bit_cast(int,v), CTRL, 0xf, 0xf, true)); }
template<int CTRL> DI u32 dppu(u32 v){ return (u32)__builtin_amdgcn_update_dpp(0, (int)v, CTRL, 0xf, 0xf, true); }
DI float sw16(float a, float b){ auto r=__builtin_amdgcn_permlane16_swap(__float_as_uint(a),__float_as_uint(b),false,false); return __uint_as_float(r[0])+__uint_as_float(r[1]); }
DI float sw32(float a, float b){ auto r=__builtin_amdgcn_permlane32_swap(__float_as_uint(a),__float_as_uint(b),false,false); return __uint_as_float(r[0])+__uint_as_float(r[1]); }
DI float wsum(float v){ v += dpp<0x121>(v); v += dpp<0x122>(v); v += dpp<0x124>(v); v += dpp<0x128>(v); v = sw16(v,v); return sw32(v,v); }
DI u32 umax(u32 a, u32 b){ return a > b ? a : b; }
DI u32 wmaxu(u32 v){
  v = umax(v,dppu<0x121>(v)); v = umax(v,dppu<0x122>(v)); v = umax(v,dppu<0x124>(v)); v = umax(v,dppu<0x128>(v));
  { auto r=__builtin_amdgcn_permlane16_swap(v,v,false,false); v=umax(r[0],r[1]); }
  { auto r=__builtin_amdgcn_permlane32_swap(v,v,false,false); v=umax(r[0],r[1]); }
  return v;
}
DI u32 pkmaxabs(u32 m, u32 v){ u32 r; asm("v_pk_maximum3_f16 %0,%1,%2,%2 neg_lo:[0,0,1] neg_hi:[0,0,1]" : "=v"(r) : "v"(m), "v"(v)); return r; }
DI float pow2i(int k){ k = k < -100 ? -100 : (k > 100 ? 100 : k); return __uint_as_float((u32)(127 + k) << 23); }
DI u32 max4(u32x4 a){ return umax(umax(umax(a.x,a.y),a.z),a.w); }
DI u32 max4m(u32 m, u32x4 a){ return umax(umax(umax(umax(m,a.x),a.y),a.z),a.w); }
DI u32 or4(u32x4 a){ return a.x | a.y | a.z | a.w; }
DI u32 and4(u32x4 a){ return a.x & a.y & a.z & a.w; }
DI u32x4 ldsc(rsrc_t rs, u32 off){ return __builtin_amdgcn_raw_buffer_load_b128(rs, off, 0, 16); }
DI void stsc(rsrc_t rs, u32 off, u32x4 v){ __builtin_amdgcn_raw_buffer_store_b128(v, rs, off, 0, 16); }
DI void stsc2(rsrc_t rs, u32 off, u32x2 v){ __builtin_amdgcn_raw_buffer_store_b64(v, rs, off, 0, 16); }
DI void stsc1(rsrc_t rs, u32 off, u32 v){ __builtin_amdgcn_raw_buffer_store_b32(v, rs, off, 0, 16); }
DI float lnrd(float sumsq){ return __builtin_amdgcn_rcpf(rbf(__builtin_amdgcn_sqrtf(rbf(rbf(sumsq*(1.f/640.f)) + 1e-5f)))); }
DI float silu_bf(u32 c16){ float cv = bflo(c16); return rbf(cv * __builtin_amdgcn_rcpf(1.f + __expf(-cv))); }
DI float red8(const float (&a)[8], int lane){
  float s0 = sw32(a[0],a[4]), s1 = sw32(a[1],a[5]), s2 = sw32(a[2],a[6]), s3 = sw32(a[3],a[7]);
  float r0 = sw16(s0,s2), r1 = sw16(s1,s3);
  float keep = (lane & 8) ? r1 : r0, give = (lane & 8) ? r0 : r1;
  return keep + dpp<0x128>(give);
}

#define FFT640(rda_, o0, o1, H0, H1, H2, H3, H4, H5, H6, H7) do { \
  u32x4 a0 = *(const u32x4*)(smd + (rda_)), a1 = *(const u32x4*)(smd + (rda_) + 32), a2 = *(const u32x4*)(smd + (rda_) + 64), a3 = *(const u32x4*)(smd + (rda_) + 96); \
  f32x16 d0, d1; u32x4 e; H0; \
  mf2z(d0, d1, a0, P0[0], a0, P1[0]); H1; mf2(d0, d1, a1, P0[1], a1, P1[1]); H2; mf2(d0, d1, a2, P0[2], a2, P1[2]); H3; mf2(d0, d1, a3, P0[3], a3, P1[3]); H4; \
  mfdone(d0, d1); \
  _Pragma("unroll") for (int j = 0; j < 4; j++) e[j] = cmul(pkh(d0[j], d1[j]), TW[0][j]); \
  mf2z(o0, o1, Q0[0], e, Q1[0], e); H5; \
  _Pragma("unroll") for (int j = 0; j < 4; j++) e[j] = cmul(pkh(d0[4+j], d1[4+j]), TW[1][j]); \
  mf2(o0, o1, Q0[1], e, Q1[1], e); H6; \
  e[0] = cmul(pkh(d0[8], d1[8]), TW[2][0]); e[1] = cmul(pkh(d0[9], d1[9]), TW[2][1]); e[2] = 0u; e[3] = 0u; \
  mf2(o0, o1, Q0[2], e, Q1[2], e); H7; \
  mfdone(o0, o1); } while (0)
#define NOH do {} while (0)
#define TWSTEP(s, PX) do { u32x4 e_; _Pragma("unroll") for (int j = 0; j < 4; j++) e_[j] = cmul(pkh(d0[4*(s)+j], d1[4*(s)+j]), TX[s][j]); \
    if ((s) == 0) mf2z(n0, n1, PX[0], e_, P1[0], e_); else mf2(n0, n1, PX[s], e_, P1[s], e_); } while (0)
#define R0 52
#define RPER 4
#define NPF2 16
#define PFC0() DMASO(HL(0), HS(0), 0)
#define PFC1() DMAC(HS(0), 1024)
#define PFC2() DMAC(HS(0), 2048)
#define PFC3() DMAC(HS(0), 3072)
#define PFC4() WLOAD(WD[0], KE(0)*5120 + 0, 0)
#define PFC5() WLOAD(WD[1], KE(0)*5120 + 0, 1024)
#define PFC6() WLOAD(WD[2], KE(0)*5120 + 0, 2048)
#define PFC7() WLOAD(WD[3], KE(0)*5120 + 0, 3072)
#define PFC8() WLOAD(WD[4], KE(0)*5120 + 4096, 0)
#define PFC9() WLOAD(WD[5], KE(1)*5120 + 0, 0)
#define PFC10() WLOAD(WD[6], KE(1)*5120 + 0, 1024)
#define PFC11() WLOAD(WD[7], KE(1)*5120 + 0, 2048)
#define PFC12() WLOAD(WD[8], KE(1)*5120 + 0, 3072)
#define PFC13() WLOAD(WD[9], KE(1)*5120 + 4096, 0)
#define PFC14() WLOAD(WD[10], KE(2)*5120 + 0, 0)
#define PFC15() WLOAD(WD[11], KE(2)*5120 + 0, 1024)
#define PFC16() WLOAD(WD[12], KE(2)*5120 + 0, 2048)
#define PFC17() WLOAD(WD[13], KE(2)*5120 + 0, 3072)
#define PFC18() WLOAD(WD[14], KE(2)*5120 + 4096, 0)
#define PFC19() WLOAD(WD[15], KE(3)*5120 + 0, 0)
#define PFC20() WLOAD(WD[16], KE(3)*5120 + 0, 1024)
#define PFC21() WLOAD(WD[17], KE(3)*5120 + 0, 2048)
#define PFC22() WLOAD(WD[18], KE(3)*5120 + 0, 3072)
#define PFC23() WLOAD(WD[19], KE(3)*5120 + 4096, 0)
#define PFC24() WLOAD(WD[20], KE(4)*5120 + 0, 0)
#define PFC25() WLOAD(WD[21], KE(4)*5120 + 0, 1024)
#define PFC26() do {} while (0)
#define PFC27() do {} while (0)
#define PFC28() do {} while (0)
#define PFC29() do {} while (0)
#define PFC30() do {} while (0)
#define PFC31() do {} while (0)

#define TABL 98304
#define GST2(v0, v1, off) asm volatile("buffer_store_dwordx2 %0,%1,%2,0 offen sc1" :: "v"(u32x2{(u32)(v0), (u32)(v1)}), "v"((u32)(off)), "s"(rs) : "memory")

#define NOUNROLL _Pragma("clang loop unroll(disable)")
#define GLD4N(d, voff, sp) asm volatile("global_load_dwordx4 %0,%1,%2 nt" : "=v"(d) : "v"(voff), "s"(sp) : "memory")
#define GLD4(d, voff, sp, imm) asm volatile("global_load_dwordx4 %0,%1,%2 offset:%3" : "=v"(d) : "v"(voff), "s"(sp), "i"(imm) : "memory")
#define GLD2(d, voff, sp, imm) asm volatile("global_load_dwordx2 %0,%1,%2 offset:%3" : "=v"(d) : "v"(voff), "s"(sp), "i"(imm) : "memory")
#define GLD1(d, voff, sp, imm) asm volatile("global_load_dword %0,%1,%2 offset:%3" : "=v"(d) : "v"(voff), "s"(sp), "i"(imm) : "memory")
#define GLDH(d, voff, sp, imm) asm volatile("global_load_ushort %0,%1,%2 offset:%3" : "=v"(d) : "v"(voff), "s"(sp), "i"(imm) : "memory")
#define GDMA(ldsaddr, voff, sp) asm volatile("s_mov_b32 m0,%0\n s_nop 0\n global_load_lds_dwordx4 %1,%2" :: "s"((u32)(ldsaddr)), "v"(voff), "s"(sp) : "memory")
#define BLD4(d, voff, soff) asm volatile("buffer_load_dwordx4 %0,%1,%2,%3 offen sc1" : "=v"(d) : "v"(voff), "s"(rs), "s"((u32)(soff)) : "memory")
#define BST4(v, voff, soff) asm volatile("buffer_store_dwordx4 %0,%1,%2,%3 offen sc1\n s_nop 1" :: "v"(v), "v"(voff), "s"(rs), "s"((u32)(soff)) : "memory")
#define BST1(v, voff, soff, imm) asm volatile("buffer_store_dword %0,%1,%2,%3 offen offset:%4 sc1" :: "v"(v), "v"(voff), "s"(rs), "s"((u32)(soff)), "i"(imm) : "memory")
#define WV(n) "s_waitcnt vmcnt(" #n ")"
#define DMASO(ldsaddr, goff, imm) asm volatile("s_mov_b32 m0,%0\n s_nop 0\n buffer_load_dwordx4 %1,%2,%3 offen offset:%4 sc1 lds" :: "s"((u32)(ldsaddr)), "v"(l16), "s"(rs), "s"((u32)(goff)), "i"(imm) : "memory")
#define DMAS(ldsaddr, goff) DMASO(ldsaddr, goff, 0)
#define DMAC(goff, imm) asm volatile("buffer_load_dwordx4 %0,%1,%2 offen offset:%3 sc1 lds" :: "v"(l16), "s"(rs), "s"((u32)(goff)), "i"(imm) : "memory")
#define ALOAD(d, off, imm) asm volatile("buffer_load_dwordx4 %0,%1,%2,%3 offen offset:%4 sc1" : "=v"(d) : "v"(l16), "s"(rs), "s"((u32)(asrc + (off))), "i"(imm) : "memory")
#define ARELOAD(d, off) asm volatile("buffer_load_dwordx4 %0,%1,%2,%3 offen sc1" : "+v"(d) : "v"(l16), "s"(rs), "s"((u32)(asrc + (off))) : "memory")
#define XLOAD(d, i, off) asm volatile("buffer_load_dwordx4 %0,%1,%2,%3 offen sc1" : "=v"(d) : "v"(xvo[i]), "s"(rs), "s"((u32)(xsrc + (off))) : "memory")
#define XRELOAD(d, i, off) asm volatile("buffer_load_dwordx4 %0,%1,%2,%3 offen sc1" : "+v"(d) : "v"(xvo[i]), "s"(rs), "s"((u32)(xsrc + (off))) : "memory")
#define WLOAD(d, off, imm) asm volatile("buffer_load_dwordx4 %0,%1,%2,%3 offen offset:%4 sc1" : "=a"(d) : "v"(l16), "s"(rs), "s"((u32)(dsrc + (off))), "i"(imm) : "memory")
#define WLOADV(d, off, imm) asm volatile("buffer_load_dwordx4 %0,%1,%2,%3 offen offset:%4 sc1" : "=v"(d) : "v"(l16), "s"(rs), "s"((u32)(dsrc + (off))), "i"(imm) : "memory")
#define LRD(d, rb, off) asm volatile("ds_read_b128 %0,%1 offset:" #off : "=v"(d) : "v"(rb) : "memory")
#define LRDA(d, rb, off) asm volatile("ds_read_b128 %0,%1 offset:" #off : "=a"(d) : "v"(rb) : "memory")
#define M16 "v_mfma_f32_16x16x32_bf16 "
#define MAZ(wt, c, wf, a) asm volatile(wt M16 "%0,%1,%2,0" : "=&a"(c) : "v"(wf), "v"(a) : "memory")
#define MA(wt, c, wf, a) asm volatile(wt M16 "%0,%1,%2,%0" : "+a"(c) : "v"(wf), "v"(a) : "memory")
#define MBZ(c, wf, a, rb, off) asm volatile(M16 "%0,%1,%2,0\n ds_read_b128 %1,%3 offset:" #off : "=&a"(c), "+v"(wf) : "v"(a), "v"(rb) : "memory")
#define MB(c, wf, a, rb, off) asm volatile(M16 "%0,%1,%2,%0\n ds_read_b128 %1,%3 offset:" #off : "+a"(c), "+v"(wf) : "v"(a), "v"(rb) : "memory")
#define MGZ(y, wf, x) asm volatile(M16 "%0,%1,%2,0" : "=&a"(y) : "a"(wf), "v"(x) : "memory")
#define MG(y, wf, x) asm volatile(M16 "%0,%1,%2,%0" : "+a"(y) : "a"(wf), "v"(x) : "memory")
#define MGV(y, wf, x) asm volatile(M16 "%0,%1,%2,%0" : "+a"(y) : "v"(wf), "v"(x) : "memory")
#define CKA(n, f, first) do { if (first) asm volatile("s_waitcnt vmcnt(" #n ")\n v_max_u32 %0,%1,%2" : "=v"(mchk) : "v"(f.x), "v"(f.y) : "memory"); \
    else asm volatile("s_waitcnt vmcnt(" #n ")\n v_max3_u32 %0,%0,%1,%2" : "+v"(mchk) : "v"(f.x), "v"(f.y) : "memory"); } while (0)
#define CKB(f) asm volatile("v_max3_u32 %0,%0,%1,%2" : "+v"(mchk) : "v"(f.z), "v"(f.w))
#define EPA(G, U, k, r) do { float t_; asm volatile("v_accvgpr_read_b32 %0,%2\n v_accvgpr_read_b32 %1,%3\n v_max_f32 %0,0,%0\n v_mul_f32 %0,%0,%1" : "=&v"(r), "=&v"(t_) : "a"(G[k]), "a"(U[k])); } while (0)
#define EPC(e) asm volatile("v_cvt_pk_bf16_f32 %0,%1,%2" : "=v"(eo[e]) : "v"(ea), "v"(eb))
#define EDMA(k) asm volatile("s_mov_b32 m0,%0\n s_nop 0\n buffer_load_dwordx4 %1,%2,%3 offen sc1 lds" :: "s"(w*4096 + (k)*1024), "v"(edo[k]), "s"(rs), "s"((k) < 3 ? x1b : gbb) : "memory")
#define YWR(y, off) asm volatile("ds_write_b128 %0,%1 offset:" #off :: "v"(rds), "a"(y) : "memory")
#define AOFF(k) (AG((k) >> 2) + ((k) & 3)*1024)
#define ROT 0
#define CHECK(g) do { if (__builtin_expect(__any(mchk == SENT), 0)) { asm volatile("s_waitcnt vmcnt(0)" ::: "memory"); NOUNROLL for (int sp = 0; sp < SPINMAX; sp++) { \
    ARELOAD(A[4*(g)], AOFF(4*(g))); ARELOAD(A[4*(g)+1], AOFF(4*(g)+1)); ARELOAD(A[4*(g)+2], AOFF(4*(g)+2)); ARELOAD(A[4*(g)+3], AOFF(4*(g)+3)); \
    asm volatile("s_waitcnt vmcnt(0)" : "+v"(A[4*(g)]), "+v"(A[4*(g)+1]), "+v"(A[4*(g)+2]), "+v"(A[4*(g)+3])); \
    if (!__any(max4m(max4m(max4m(max4(A[4*(g)]),A[4*(g)+1]),A[4*(g)+2]),A[4*(g)+3]) == SENT)) break; } } } while (0)
#define EPIS(j) asm volatile("buffer_store_dwordx4 %0,%1,%2,%3 offen offset:%4 sc1\n s_nop 1" :: "v"(u32x4{eo[0], eo[1], eo[2], eo[3]}), "v"(l16), "s"(rs), "s"(hob + ((j) >> 1)*4096), "i"(((j) & 1)*1024) : "memory")
#define TILEDMA(job) do { \
      if ((job) < 160) { const u16* wp = ((w & 1) ? wu : wg) + ((job) >> 3)*32*1536 + ((job) & 7)*192; \
        _Pragma("unroll") for (u32 i = 0; i < 12; i++) { u32 u = 64*i + lane, row = u / 24; GDMA(tlb + i*1024, row*3072 + (u - row*24)*16, wp); } \
      } else { const u16* wp = wd + (((job) - 160) >> 1)*32*640 + ((job) & 1)*320 + (w & 1)*160; \
        _Pragma("unroll") for (u32 i = 0; i < 10; i++) { u32 u = 64*i + lane, row = u / 20; GDMA(tlb + i*1024, row*1280 + (u - row*20)*16, wp); } \
      } } while (0)
#define TR12(base, o0, o1, o2, o3, o4, o5, rs_) asm volatile( \
        "ds_read_b64_tr_b16 %0,%12 offset:%13\n ds_read_b64_tr_b16 %1,%12 offset:%13+%19\n ds_read_b64_tr_b16 %2,%12 offset:%14\n ds_read_b64_tr_b16 %3,%12 offset:%14+%19\n" \
        "ds_read_b64_tr_b16 %4,%12 offset:%15\n ds_read_b64_tr_b16 %5,%12 offset:%15+%19\n ds_read_b64_tr_b16 %6,%12 offset:%16\n ds_read_b64_tr_b16 %7,%12 offset:%16+%19\n" \
        "ds_read_b64_tr_b16 %8,%12 offset:%17\n ds_read_b64_tr_b16 %9,%12 offset:%17+%19\n ds_read_b64_tr_b16 %10,%12 offset:%18\n ds_read_b64_tr_b16 %11,%12 offset:%18+%19\n s_waitcnt lgkmcnt(0)" \
        : "=&v"(f[0]), "=&v"(f[1]), "=&v"(f[2]), "=&v"(f[3]), "=&v"(f[4]), "=&v"(f[5]), "=&v"(f[6]), "=&v"(f[7]), "=&v"(f[8]), "=&v"(f[9]), "=&v"(f[10]), "=&v"(f[11]) \
        : "v"(base), "i"(o0), "i"(o1), "i"(o2), "i"(o3), "i"(o4), "i"(o5), "i"(rs_) : "memory")
#define FRG(i) (u32x4{f[2*(i)].x, f[2*(i)].y, f[2*(i)+1].x, f[2*(i)+1].y})
#define TILEJOB(job) do { \
      const u32 G = lane >> 4, q = (lane & 15) >> 2, p = lane & 3; u32x2 f[12]; \
      if ((job) < 160) { \
        const u32 base = tlb + (8*G + q)*384 + p*16, ks = (job) >> 3; \
        _Pragma("unroll") for (int hh = 0; hh < 2; hh++) { \
          TR12(base + hh*192, 0, 8, 64, 72, 128, 136, 1536); \
          const u32 ob = WS_WT1 + (((job) & 7)*2 + hh)*245760 + (w & 1)*2048; \
          _Pragma("unroll") for (int i = 0; i < 6; i++) BST4(FRG(i), l16, ob + (i < 4 ? 8*ks + 4*(i >> 1) + (i & 1) : 160 + 4*ks + (i & 1))*1024); } \
      } else { \
        const u32 base = tlb + (8*G + q)*320 + p*40; \
        _Pragma("unroll") for (int hh = 0; hh < 2; hh++) { \
          TR12(base + hh*160, 0, 8, 16, 24, 32, 32, 1280); \
          const u32 ob = WS_WT2 + ((((job) & 1)*4 + (w & 1)*2 + hh)*48 + (((job) - 160) >> 1))*5120; \
          _Pragma("unroll") for (int i = 0; i < 5; i++) BST4(FRG(i), l16, ob + i*1024); } \
      } } while (0)
#define H2FILL() do { _Pragma("unroll") for (int i = 0; i < 5; i++) BST4(sv, h2o - 8192, h2b + i*8192); } while (0)
#define HTF(i) BST4(sv, l16, hob + ((i) >> 1)*4096 + ((i) & 1)*1024)
#define GFPUB() do { if (t == 0) GST2(0, tag, WS_GF + a*8); else asm volatile("s_nop 0"); } while (0)
#define HTFILL() do { BST4(sv, l16, hob); BST4(sv, l16, hob + 1024); BST4(sv, l16, hob + 4096); BST4(sv, l16, hob + 5120); BST4(sv, l16, hob + 8192); BST4(sv, l16, hob + 9216); } while (0)
#define TILE 65536
#define LNPREP() do { \
    { const u32x4* tb = (const u32x4*)(smd + TABL) + lane; \
      _Pragma("unroll") for (int s = 0; s < 4; s++) P0[s] = tb[s*64]; \
      _Pragma("unroll") for (int s = 0; s < 3; s++) { Q0[s] = tb[(4 + s)*64]; TW[s] = tb[(7 + s)*64]; } } \
    _Pragma("unroll") for (int i = 0; i < 5; i++) { \
      float a0_, a1_, b0_, b1_;        \
      asm volatile("v_lshlrev_b32 %0,16,%6\n v_lshlrev_b32 %1,16,%7\n v_and_b32 %2,0xffff0000,%6\n v_and_b32 %3,0xffff0000,%7\n v_perm_b32 %4,%7,%6,%8\n v_perm_b32 %5,%7,%6,%9" \
                   : "=&v"(a0_), "=&v"(a1_), "=&v"(b0_), "=&v"(b1_), "=&v"(xq[2*i]), "=&v"(xq[2*i + 1]) : "v"(i < 4 ? x0[i] : x0b), "v"(i < 4 ? x1[i] : x1b), "s"(0x05040100u), "s"(0x07060302u)); \
      v[2*i] = f32x2{a0_, a1_}; v[2*i+1] = f32x2{b0_, b1_}; } \
    asm volatile("" :: "v"(cpf), "v"(tlw)); \
    f32x2 sum = ((v[0] + v[1]) + (v[2] + v[3])) + ((v[4] + v[5]) + (v[6] + v[7])) + (v[8] + v[9]); \
    f32x2 mean = f32x2{wsum(sum.x), wsum(sum.y)} * (1.f/640.f); \
    f32x2 sq = {0.f, 0.f}; \
    _Pragma("unroll") for (int i = 0; i < 10; i++) { v[i] -= mean; sq += v[i]*v[i]; } \
    f32x2 rd = {lnrd(wsum(sq.x)), lnrd(wsum(sq.y))}; \
    _Pragma("unroll") for (int i = 0; i < 10; i++) v[i] *= rd; \
    _Pragma("unroll") for (int s = 0; s < 4; s++) P1[s] = rot90x4(P0[s]); \
    _Pragma("unroll") for (int s = 0; s < 3; s++) Q1[s] = rot90x4(Q0[s]); \
    _Pragma("unroll") for (int i = 0; i < 10; i++) asm volatile("" : "+v"(v[i])); \
    _Pragma("unroll") for (int s = 0; s < 3; s++) asm volatile("" : "+v"(P1[s]), "+v"(Q1[s]), "+v"(TW[s])); \
    asm volatile("" : "+v"(P1[3])); } while (0)
#define XQPREP() do { u32* xb = (u32*)scr + w*720; \
    _Pragma("unroll") for (int i = 0; i < 5; i++) \
      *(u32x2*)(xb + lane*10 + 2*i) = u32x2{xq[2*i], xq[2*i + 1]}; \
    _Pragma("unroll") for (int i = 0; i < 10; i++) xq[i] = xb[(lane & 31) + 32*K2MAP(i, lane >> 5)]; \
    _Pragma("unroll") for (int i = 0; i < 10; i++) asm volatile("" : "+v"(xq[i])); } while (0)
#define FILLT() do { const u32 uo = t16 - 2048, tb = WS_T1 + a*TROW*16, u5 = t < 136 ? uo + 10240 : uo; \
      _Pragma("unroll") for (int i = 0; i < 5; i++) BST4(sv, uo, tb + i*2048); \
      BST4(sv, u5, tb); } while (0)
#define K2MAP(i, h) ((i) < 8 ? ((i) & 3) + 8*((i) >> 2) + 4*(h) : 8 + (i) + 2*(h))

#define MODDELAY 90
#define CANDELAY 130
#define GWDELAY 8
#define LSLEEP() __builtin_amdgcn_s_sleep(8)
#define SLEEP() __builtin_amdgcn_s_sleep(2)
__global__ __launch_bounds__(256, 1) void fused(const u16* __restrict__ x, const u16* __restrict__ wr, const u16* __restrict__ wi,
    const u16* __restrict__ wg, const u16* __restrict__ wu, const u16* __restrict__ wd, const u16* __restrict__ cvec, const u16* __restrict__ wm,
    char* __restrict__ ws, u16* __restrict__ out){
  extern __shared__ __attribute__((aligned(16))) char smd[];
  const u32 t = threadIdx.x & 255, lane = t & 63, w = __builtin_amdgcn_readfirstlane(t >> 6), blk = blockIdx.x & 255;
  const u32 xc = blk & 7, rr_ = blk >> 3;
  const u32 pm = (xc >> 2)*8 + (rr_ & 7), pn = (xc & 3)*4 + (rr_ >> 3), a = pm*16 + pn;
  const rsrc_t rs = __builtin_amdgcn_make_buffer_rsrc(ws, 0, WS_END, 0x00020000);
  const u32 l16 = lane*16, t16 = t*16;
  char* scr = smd + SCR;
  u32 ep;
  asm volatile("buffer_load_dword %0,off,%1,0 sc1" : "=v"(ep) : "s"(rs) : "memory");
  const u32 gidx = xc*20 + rr_;
  const u32 rot = blk < 160 ? 80*(gidx & 7) : 0;
  u32 r0 = t + rot, r1 = r0 + 256, r2 = t < 128 ? r0 + 512 : r0;
  r0 = r0 >= 640 ? r0 - 640 : r0; r1 = r1 >= 640 ? r1 - 640 : r1; r2 = r2 >= 640 ? r2 - 640 : r2;
  u32x4 gv0, gv1, gv2; u32 gc0, gc1, gc2;
  u32x2 wl[16];
  const u32 tau = blk - 160, wth = tau >> 5, wu_ = (tau >> 3) & 3, wc_ = tau & 7;
  if (blk < 160) { const u16* wp = wm + gidx*8; GLD4(gv0, r0*7680, wp, 0); GLD4(gv1, r1*7680, wp, 0); GLD4(gv2, r2*7680, wp, 0); }
  else {
    const u32 vo = ((t >> 5)*321 + ((t & 31) >= 26 ? 103 : (t & 31)*4))*2, so = (8*wu_ + 128*wc_)*321 + wth*107;
    #pragma unroll
    for (int i = 0; i < 16; i++) GLD2(wl[i], vo, ((i & 8) ? wi : wr) + so + ((i & 3)*32 + (i & 4)*256)*321, 0);
  }
  GLDH(gc0, r0*2, cvec, 0); GLDH(gc1, r1*2, cvec, 0); GLDH(gc2, r2*2, cvec, 0);
  const u32 m = 32*(4*(a >> 5) + w) + (a & 31);
  u32x4 x0, x1; u32 x0b, x1b;
  { const u16* xr = x + (size_t)m*1280; GLD4(x0, lane*20, xr, 0); GLD4(x1, lane*20, xr, 1280); GLD1(x0b, lane*20, xr, 16); GLD1(x1b, lane*20, xr, 1296); }
  { const u32* tb = TAB + w*1024; GDMA(TABL + w*4096, l16, tb); GDMA(TABL + w*4096 + 1024, l16, tb + 256); GDMA(TABL + w*4096 + 2048, l16, tb + 512); GDMA(TABL + w*4096 + 3072, l16, tb + 768);
    GDMA(TABL + (16 + (w & 1))*1024, l16, TAB + (16 + (w & 1))*256); }
  u32 tlw;
  {
    const char* pp = ws + (rr_ < 14 ? (rr_ < 13 ? rr_*2097152u : (u32)WS_END - 4u) : 0u);
    if (rr_ >= 14) {
      const u32 q = rr_ - 14, b_ = q / 3, k_ = q - 3*b_;
      const char* bs = b_ == 0 ? (const char*)out : (b_ == 1 ? (const char*)wg : (b_ == 2 ? (const char*)wu : (b_ == 3 ? (const char*)wd : (b_ == 4 ? (const char*)wm : (const char*)x))));
      const u32 sz = b_ == 4 ? 4915200u : (b_ == 0 || b_ == 5 ? 2621440u : 1966080u);
      pp = bs + (k_ == 0 ? 0u : (k_ == 1 ? (sz >> 1) : sz - 4u));
    }
    asm volatile("global_load_dword %0,%1,%2" : "=v"(tlw) : "v"(0u), "s"(pp) : "memory");
  }
  u32 cpf;
  {
    const char* pc; u32 clen;
    asm volatile("s_getpc_b64 %0\nfused_code_here:\n s_mov_b32 %1, fused_code_end - fused_code_here" : "=s"(pc), "=s"(clen));
    u32 co = (rr_*16 + (lane & 15))*128; co = co < clen ? co : 0;
    asm volatile("global_load_dword %0,%1,%2" : "=v"(cpf) : "v"(co), "s"(pc) : "memory");
  }
  if (blk >= 160) {
    const u32x4 sv = {SENT, SENT, SENT, SENT};
    const u32 tb = WS_T1 + a*TROW*16, s3 = t < 136 ? t16 + 8192 : t16;
    BST4(sv, t16, tb); BST4(sv, t16, tb + 4096); BST4(sv, s3, tb);
  }
  const u32 hob = WS_HT + (((2*pm + (w >> 1))*48 + pn*3)*4 + 2*(w & 1))*1024;
  const u32 h2o = ((t >> 5)*128 + ((t >> 3) & 3)*16 + (t & 7))*16, h2b = WS_H2 + (a >> 2)*40960 + ((a >> 1) & 1)*1024 + (a & 1)*128;
  const u32x4 sv = {SENT, SENT, SENT, SENT};
  const u32 tlb = w >= 2 ? TILE + (w & 1)*12288 : 40960 + (w & 1)*12288;
  u32 wla = ((t >> 5)*27 + ((t & 31) >= 26 ? 26 : (t & 31)))*8;
  const u32 kl3 = (t >> 3) < 11 ? (t >> 3) + 96 : 106;
  u32 qa = ((t & 7)*108 + (t >> 3))*2, qa3 = ((t & 7)*108 + kl3 + (kl3 >= 104 ? 1 : 0))*2, wo = (t >> 3)*WPS + (t & 7)*16, wo3 = kl3*WPS + (t & 7)*16;
  u32 ra[16], rb_[16];
  #pragma unroll
  for (int e = 0; e < 16; e++) { ra[e] = 0u; rb_[e] = 0u; asm volatile("" : "+v"(ra[e]), "+v"(rb_[e])); }
  asm volatile("" : "+v"(qa), "+v"(qa3), "+v"(wo), "+v"(wo3), "+v"(wla));
  u32x4 P0[4], P1[4], Q0[3], Q1[3], TW[3], TX[4];
  u32x4 g0, g1, g2;
  const u32 u0 = 160*w + lane, u2 = lane < 32 ? u0 + 128 : u0;
#define MODLOADS(CON, TAIL) asm volatile("buffer_load_dwordx4 %0,%3,%5,%6 offen sc1\n buffer_load_dwordx4 %1,%3,%5,%6 offen offset:1024 sc1\n buffer_load_dwordx4 %2,%4,%5,%6 offen sc1" TAIL \
      : CON(g0), CON(g1), CON(g2) : "v"(u0*16), "v"(u2*16), "s"(rs), "s"((u32)WS_GM) : "memory")
  u32 xq[10], gwv = 0, gwo = WS_GW + (96 + blk)*8; f32x2 v[10];
  asm volatile("s_cmp_ge_u32 %4,160\n s_cbranch_scc1 1f\n s_waitcnt vmcnt(11)\n1: s_waitcnt vmcnt(14)" : "+v"(ep), "+v"(gc0), "+v"(gc1), "+v"(gc2) : "s"(blk) : "scc", "memory");
  const u32 tag = __builtin_amdgcn_readfirstlane(ep) + 1u;
  float* sil = (float*)(scr + 33792);
  if (blk < 160) {
    asm volatile("" : "+v"(gv0), "+v"(gv1), "+v"(gv2));
    float s0 = silu_bf(gc0), s1 = silu_bf(gc1), s2 = t < 128 ? silu_bf(gc2) : 0.f;
    float ac[8];
    #pragma unroll
    for (int j = 0; j < 4; j++) { ac[2*j] = s0*bflo(gv0[j]) + s1*bflo(gv1[j]) + s2*bflo(gv2[j]); ac[2*j+1] = s0*bfhi(gv0[j]) + s1*bfhi(gv1[j]) + s2*bfhi(gv2[j]); }
    float gs = red8(ac, lane);
    gs += dpp<0x141>(gs); gs += dpp<0xB1>(gs); gs += dpp<0x4E>(gs);
    float* red = (float*)(scr + 32768);
    if ((lane & 7) == 0) red[w*8 + (lane >> 3)] = gs;
    LBAR();
    if (t < 4) {
      float v0 = red[2*t] + red[8 + 2*t] + red[16 + 2*t] + red[24 + 2*t], v1 = red[2*t + 1] + red[9 + 2*t] + red[17 + 2*t] + red[25 + 2*t];
      u32 p = pkbf(v0, v1);
      if (gidx >= 80) p = pkbf(1.f + bflo(p), 1.f + bfhi(p));
      GST2(p, tag, WS_GM + (gidx*4 + t)*8);
    }
    const u32 tpub = (u32)__builtin_amdgcn_s_memrealtime();
    asm volatile("s_cmp_eq_u32 %0,0\n s_cbranch_scc1 1f\n s_waitcnt vmcnt(0)\n1: s_waitcnt vmcnt(1)\n s_barrier" :: "s"(w) : "scc", "memory");
    if (w >= 2) {
      TILEDMA(blk);
      FILLT();
    }
    LNPREP();
    if (w >= 2) {
      asm volatile(WV(6) ::: "memory");
      TILEJOB(blk);
      asm volatile(WV(12) ::: "memory");
    }
    if (w < 2) {
      while ((u32)__builtin_amdgcn_s_memrealtime() - tpub < MODDELAY) __builtin_amdgcn_s_sleep(1);
      MODLOADS("=&v", "");
    }
  } else {
    asm volatile("" : "+v"(wl[0]), "+v"(wl[1]), "+v"(wl[2]), "+v"(wl[3]), "+v"(wl[4]), "+v"(wl[5]), "+v"(wl[6]), "+v"(wl[7]),
                      "+v"(wl[8]), "+v"(wl[9]), "+v"(wl[10]), "+v"(wl[11]), "+v"(wl[12]), "+v"(wl[13]), "+v"(wl[14]), "+v"(wl[15]));
    u32 mx = 0;
    #pragma unroll
    for (int i = 0; i < 16; i++) { *(u32x2*)(smd + wla + i*1728) = wl[i]; MEMFENCE(); mx = pkmaxabs(pkmaxabs(mx, wl[i].x), wl[i].y); }
    mx = wmaxu(umax(mx & 0x7fffu, (mx >> 16) & 0x7fffu));
    u32* red = (u32*)(scr + 32768);
    red[w] = mx;
    const u32 wb = WS_WP + wth*107*WPS + (wc_ >> 1)*1024 + ((wc_ & 1)*32 + 8*wu_)*16;
    asm volatile(WV(3) "\n s_waitcnt lgkmcnt(0)\n s_barrier" ::: "memory");
#define RD16(r_, q_, o_) asm volatile("ds_read_u16_d16_hi %0,%16 offset:%17\n ds_read_u16_d16_hi %1,%16 offset:%17+13824\n ds_read_u16_d16_hi %2,%16 offset:%17+6912\n ds_read_u16_d16_hi %3,%16 offset:%17+20736\n" \
                   "ds_read_u16_d16_hi %4,%16 offset:%17+1728\n ds_read_u16_d16_hi %5,%16 offset:%17+15552\n ds_read_u16_d16_hi %6,%16 offset:%17+8640\n ds_read_u16_d16_hi %7,%16 offset:%17+22464\n" \
                   "ds_read_u16_d16_hi %8,%16 offset:%17+3456\n ds_read_u16_d16_hi %9,%16 offset:%17+17280\n ds_read_u16_d16_hi %10,%16 offset:%17+10368\n ds_read_u16_d16_hi %11,%16 offset:%17+24192\n" \
                   "ds_read_u16_d16_hi %12,%16 offset:%17+5184\n ds_read_u16_d16_hi %13,%16 offset:%17+19008\n ds_read_u16_d16_hi %14,%16 offset:%17+12096\n ds_read_u16_d16_hi %15,%16 offset:%17+25920" \
                   : "+v"(r_[0]), "+v"(r_[1]), "+v"(r_[2]), "+v"(r_[3]), "+v"(r_[4]), "+v"(r_[5]), "+v"(r_[6]), "+v"(r_[7]), \
                     "+v"(r_[8]), "+v"(r_[9]), "+v"(r_[10]), "+v"(r_[11]), "+v"(r_[12]), "+v"(r_[13]), "+v"(r_[14]), "+v"(r_[15]) : "v"(q_), "i"(o_) : "memory")
#define WT16(r_) asm volatile("s_waitcnt lgkmcnt(0)" : "+v"(r_[0]), "+v"(r_[1]), "+v"(r_[2]), "+v"(r_[3]), "+v"(r_[4]), "+v"(r_[5]), "+v"(r_[6]), "+v"(r_[7]), \
                     "+v"(r_[8]), "+v"(r_[9]), "+v"(r_[10]), "+v"(r_[11]), "+v"(r_[12]), "+v"(r_[13]), "+v"(r_[14]), "+v"(r_[15]) :: "memory")
#define COEF(r_, vo_, so_) do { u32x4 os, od; \
      _Pragma("unroll") for (int e = 0; e < 4; e++) { \
        const f32x2 av = {__uint_as_float(r_[4*e]), __uint_as_float(r_[4*e + 1])}, bv = {__uint_as_float(r_[4*e + 2]), __uint_as_float(r_[4*e + 3])}; \
        const f32x2 S = (av + bv)*kap, Dd = (av - bv)*kap; os[e] = pkh(S.x, S.y); od[e] = pkh(Dd.x, Dd.y); } \
      BST4(os, vo_, so_); BST4(od, vo_, (so_) + 4096); } while (0)
    const u32 emx = max4(*(const u32x4*)red);
    RD16(ra, qa, 0);
    const float kap = 0.5f*pow2i(-__builtin_amdgcn_frexp_expf(bflo(emx)*1.4143f));
    WT16(ra); RD16(rb_, qa, 64); COEF(ra, wo, wb);
    WT16(rb_); RD16(ra, qa, 128); COEF(rb_, wo, wb + 32*WPS);
    WT16(ra); RD16(rb_, qa3, 0); COEF(ra, wo, wb + 64*WPS);
    WT16(rb_); COEF(rb_, wo3, wb);
    gwv = 1000 + __builtin_amdgcn_frexp_expf(bflo(emx)*1.4143f); gwo = WS_GW + (wth*32 + wu_*8 + (wc_ & 1)*4 + (wc_ >> 1))*8;
    LNPREP();
    sil[r0] = silu_bf(gc0); sil[r1] = silu_bf(gc1); sil[r2] = silu_bf(gc2);
    if (w < 2) MODLOADS("=&v", "");
  }
  float s1sc;
  u32 fra = SCR + w*2880 + ((lane & 31) - 2*((lane & 31) >= 18) - 10*((lane & 31) >= 22))*144 + (lane >> 5)*16;
  const u32 hx = blk - 161;
  {
    u32* modb = (u32*)(scr + 24576);
    u32 cs0, cs1, cs2, wra, rda, xwa;
    { u32 k0 = t >> 1, k1 = k0 + 128, k2 = t < 132 ? (k0 + 256 > 320 ? 320 : k0 + 256) : k0;
      if (t & 1) { k0 = k0 ? 640 - k0 : 0; k1 = 640 - k1; k2 = 640 - k2; }
      cs0 = SCR + 11520 + k0*16; cs1 = SCR + 11520 + k1*16; cs2 = SCR + 11520 + k2*16;
      wra = SCR + w*2880 + (lane & 1)*1440 + (lane >> 1)*4; rda = SCR + 24576 + lane*20; xwa = SCR + 11520 + (lane & 31)*16 + w*4; }
    const u32 s3b = t < 132 ? t16 + 8192 : t16;
    asm volatile("" : "+v"(cs0), "+v"(cs1), "+v"(cs2), "+v"(wra), "+v"(rda), "+v"(xwa), "+v"(fra));
    u32 tch0 = (lane / 5)*7680 + (lane % 5)*128, tch1 = lane < 16 ? ((lane + 64) / 5)*7680 + ((lane + 64) % 5)*128 : tch0;
    asm volatile("" : "+v"(gwv), "+v"(gwo), "+v"(tch0), "+v"(tch1));
    if (w < 2) {
      asm volatile(WV(0) : "+v"(g0), "+v"(g1), "+v"(g2) :: "memory");
      NOUNROLL for (int sp = 0; sp < SPINMAX; sp++) {
        if (__all((g0.y ^ tag | g0.w ^ tag | g1.y ^ tag | g1.w ^ tag | g2.y ^ tag | g2.w ^ tag) == 0)) break;
        SLEEP();
        MODLOADS("+v", "\n s_waitcnt vmcnt(0)");
      }
      *(u32x2*)(modb + u0*2) = u32x2{g0.x, g0.z}; *(u32x2*)(modb + u0*2 + 128) = u32x2{g1.x, g1.z}; *(u32x2*)(modb + u2*2) = u32x2{g2.x, g2.z};
      u32 mx = pkmaxabs(pkmaxabs(pkmaxabs(pkmaxabs(pkmaxabs(pkmaxabs(0u, g0.x), g0.z), g1.x), g1.z), g2.x), g2.z);
      mx = wmaxu(umax(mx & 0x7fffu, (mx >> 16) & 0x7fffu));
      modb[640 + w] = mx;
    } else if (blk >= 160) asm volatile(WV(0) ::: "memory");
    LBAR();
    if (t == 192) GST2(gwv, tag, gwo);
    if (w >= 2 && blk < 160) {
      const u16* mp = wm + 1280 + xc*320 + (rr_*32 + (w - 2)*16)*3840;
      asm volatile("s_mov_b32 m0,%0\n s_nop 0\n global_load_lds_dword %1,%3\n global_load_lds_dword %2,%3" :: "s"((u32)(TILE + 24576)), "v"(tch0), "v"(tch1), "s"(mp) : "memory");
    }
    u32 ms[5], mg[5];
    #pragma unroll
    for (int i = 0; i < 5; i++) { ms[i] = *(const u32*)(smd + rda + i*4); mg[i] = *(const u32*)(smd + rda + 1280 + i*4); }
    u32x2 mm = *(const u32x2*)(modb + 640);
    s1sc = pow2i(15 - __builtin_amdgcn_frexp_expf(bflo(umax(mm.x, mm.y))*2330.f));
    #pragma unroll
    for (int i = 0; i < 10; i++) {
      float g = (i & 1) ? bfhi(mg[i >> 1]) : bflo(mg[i >> 1]), sh = (i & 1) ? bfhi(ms[i >> 1]) : bflo(ms[i >> 1]);
      f32x2 hv = (v[i]*g + sh)*s1sc; *(u32*)(smd + wra + i*144) = pkh(hv.x, hv.y);
    }
    f32x16 o0, o1;
    FFT640(fra, o0, o1, NOH, NOH, NOH, NOH, NOH, NOH, NOH, NOH);
    #pragma unroll
    for (int i = 0; i < 10; i++) *(u32*)(smd + xwa + K2MAP(i, lane >> 5)*512) = pkh(o0[i], o1[i]) & 0xfffeffffu;
    asm volatile(WV(2) "\n s_waitcnt lgkmcnt(0)\n s_barrier" ::: "memory");
    u32x4 d0 = *(const u32x4*)(smd + cs0), d1 = *(const u32x4*)(smd + cs1), d2 = *(const u32x4*)(smd + cs2);
    const u32 tb = WS_T1 + a*TROW*16;
    BST4(d0, t16, tb); BST4(d1, t16, tb + 4096); BST4(d2, s3b, tb);
  }
  const u32 wtb = WS_WT1 + pn*245760;
#define RB(i) do { if (((i) & 3) == 0) DMAS(49152 + w*18432 + (i)*1024, wtb + 49152 + w*18432 + (i)*1024); else DMAC(wtb + 49152 + w*18432 + ((i) & ~3)*1024, ((i) & 3)*1024); } while (0)
  int eglob;
  if (blk <= 160) {
    const u32 cb = blk < 160 ? gidx : 160;
    u32 k = 2*cb + (w >> 1); k = k > 320 ? 320 : k;
    u32x4 TH[4], PA_[4], PB_[4], PC_[4];
#define BPREP() do { XQPREP(); const u32x4* tb = (const u32x4*)(smd + TABL) + lane; \
      const u32 c1 = (w & 1) ? 0x80000000u : 0u, c2 = (w & 1) ? 0x80008000u : 0u; \
      _Pragma("unroll") for (int s = 0; s < 4; s++) { TX[s] = tb[(10 + s)*64]; TH[s] = tb[(14 + s)*64] ^ c2; PA_[s] = P0[s] ^ c1; PB_[s] = P1[s] ^ c1; PC_[s] = P0[s] ^ c2; \
        asm volatile("" : "+v"(TX[s]), "+v"(TH[s]), "+v"(PA_[s]), "+v"(PB_[s]), "+v"(PC_[s])); } } while (0)
    u32x4 own[4], crs[4];
    const u32 fo = ((k >= 214 ? 64 : (k >= 107 ? 32 : 0)) + ((lane & 31) >> 3)*8 + (lane >> 5)*4)*8;
    const u32 t1o = ((64*w + (lane >> 2))*TROW + 4*cb + ((lane & 3) ^ ((lane >> 4) & 3)))*16;
    u32 lwa = w*4096 + l16, lra = lane*64 + ((w ^ ((lane >> 2) & 3))*16), xca = 16384 + w*4096 + l16;
    asm volatile("" : "+v"(lwa), "+v"(lra), "+v"(xca));
    u32x4 d[4];
#define T1LOADS(CON, TAIL) asm volatile("buffer_load_dwordx4 %0,%4,%5,%6 offen sc1\n buffer_load_dwordx4 %1,%4,%5,%7 offen sc1\n buffer_load_dwordx4 %2,%4,%5,%8 offen sc1\n buffer_load_dwordx4 %3,%4,%5,%9 offen sc1" TAIL \
      : CON(d[0]), CON(d[1]), CON(d[2]), CON(d[3]) : "v"(t1o), "s"(rs), \
        "s"((u32)WS_T1), "s"((u32)(WS_T1 + 16*TROW*16)), "s"((u32)(WS_T1 + 32*TROW*16)), "s"((u32)(WS_T1 + 48*TROW*16)) : "memory")
    u32 uneq;
    {
      u32x4 ga_, gb_;
      __builtin_amdgcn_s_sleep(GWDELAY);
      NOUNROLL for (int sp = 0; sp < SPINMAX; sp++) {
        asm volatile("buffer_load_dwordx4 %0,%2,%3,%4 offen sc1\n buffer_load_dwordx4 %1,%2,%3,%4 offen offset:1024 sc1\n s_waitcnt vmcnt(0)"
                     : "=&v"(ga_), "=&v"(gb_) : "v"(l16), "s"(rs), "s"((u32)WS_GW) : "memory");
        if (__all((ga_.y ^ tag | ga_.w ^ tag | gb_.y ^ tag | gb_.w ^ tag) == 0)) break;
        LSLEEP();
      }
      T1LOADS("=&v", "");
      { const u32 so = WS_WP + k*WPS + (w & 1)*4096;
        DMAS(32768 + w*4096, so); DMAC(so, 1024); DMAC(so, 2048); DMAC(so, 3072); }
      eglob = (int)wmaxu(umax(ga_.x, ga_.z));
      uneq = __any(lane < 48 && ((ga_.x ^ eglob) | (ga_.z ^ eglob)) != 0);
      BST4(sv, h2o, h2b); BST4(sv, h2o, h2b + 16384); BST4(sv, t < 128 ? h2o + 32768 : h2o, h2b);
      BPREP();
      asm volatile(WV(7) : "+v"(d[0]), "+v"(d[1]), "+v"(d[2]), "+v"(d[3]) :: "memory");
      NOUNROLL for (int sp = 0; sp < SPINMAX; sp++) {
        if (!__any(((or4(d[0]) | or4(d[1]) | or4(d[2]) | or4(d[3])) & 0x10000u) != 0)) break;
        LSLEEP();
        T1LOADS("+v", "\n s_waitcnt vmcnt(0)");
      }
      #pragma unroll
      for (int i = 0; i < 4; i++) *(u32x4*)(smd + lwa + i*1024) = d[i];
    }
    asm volatile(WV(3) "\n s_waitcnt lgkmcnt(0)\n s_barrier" ::: "memory");
    if (t == 192) GST2(0, tag, WS_GD + blk*8);
    f32x16 d0, d1, n0, n1;
    {
      u32x4 a0 = *(const u32x4*)(smd + lra), a1 = *(const u32x4*)(smd + lra + 4096), a2 = *(const u32x4*)(smd + lra + 8192), a3 = *(const u32x4*)(smd + lra + 12288);
      RB(0); RB(1); RB(2);
      mf2z(d0, d1, a0, PA_[0], a0, PB_[0]); RB(3); mf2(d0, d1, a1, PA_[1], a1, PB_[1]); RB(4); mf2(d0, d1, a2, PA_[2], a2, PB_[2]); RB(5); mf2(d0, d1, a3, PA_[3], a3, PB_[3]);
    }
    #pragma unroll
    for (int s = 0; s < 4; s++) { own[s] = *(const u32x4*)(smd + 32768 + (w >> 1)*8192 + s*1024 + l16); crs[s] = *(const u32x4*)(smd + 32768 + 4096 + (w >> 1)*8192 + s*1024 + l16); }
    if (uneq) {
      u32x4 f0, f1;
      asm volatile("buffer_load_dwordx4 %0,%2,%3,%4 offen sc1\n buffer_load_dwordx4 %1,%2,%3,%4 offen offset:16 sc1\n s_waitcnt vmcnt(0)" : "=&v"(f0), "=&v"(f1) : "v"(fo), "s"(rs), "s"((u32)WS_GW) : "memory");
      const u32 ee[4] = {f0.x, f0.z, f1.x, f1.z};
      #pragma unroll
      for (int s = 0; s < 4; s++) {
        const int dd = 15 + (int)ee[s] - eglob; const u32 fh = dd > 0 ? (u32)dd*0x04000400u : 0u;
        #pragma unroll
        for (int j = 0; j < 4; j++) { own[s][j] = asu(ash(own[s][j])*ash(fh)); crs[s][j] = asu(ash(crs[s][j])*ash(fh)); }
      }
    }
    #pragma unroll
    for (int s = 0; s < 4; s++)
      #pragma unroll
      for (int j = 0; j < 4; j++) {
        u32 o_ = own[s][j], c_;
        asm volatile("v_pk_fma_f16 %0,%2,%3,%0 op_sel:[0,1,0] neg_lo:[1,0,0] neg_hi:[1,0,0]\n v_pk_mul_f16 %1,%2,%3 op_sel:[1,0] op_sel_hi:[0,0] neg_lo:[1,0]" : "+v"(o_), "=&v"(c_) : "v"(crs[s][j]), "v"(TH[s][j]));
        own[s][j] = o_; crs[s][j] = c_;
      }
    asm volatile("" : "+v"(d0), "+v"(d1));
    TWSTEP(0, P0); TWSTEP(1, P0); TWSTEP(2, P0); TWSTEP(3, P0); RB(6); RB(7);
    mfdone9(n0, n1);
    u32x4 e[4];
    #pragma unroll
    for (int s = 0; s < 4; s++) {
      #pragma unroll
      for (int j = 0; j < 4; j++) e[s][j] = pkh(n1[4*s + j], n0[4*s + j]);
      *(u32x4*)(smd + xca + s*1024) = e[s];
    }
    LBAR();
    u32x4 ot[4];
    #pragma unroll
    for (int s = 0; s < 4; s++) ot[s] = *(const u32x4*)(smd + (xca ^ 4096) + s*1024);
    RB(8); RB(9); RB(10);
#define MIDSTEP(s) do { u32x4 u; _Pragma("unroll") for (int j = 0; j < 4; j++) u[j] = cmul2(e[s][j], own[s][j], ot[s][j], crs[s][j]); \
      if ((s) == 0) mf2z(d0, d1, u, P0[0], u, P1[0]); else mf2(d0, d1, u, P0[s], u, P1[s]); } while (0)
    MIDSTEP(0); MIDSTEP(1); MIDSTEP(2); MIDSTEP(3); RB(11); RB(12);
    mfdone9(d0, d1);
    TWSTEP(0, PC_); TWSTEP(1, PC_); TWSTEP(2, PC_); TWSTEP(3, PC_); RB(13); RB(14);
    mfdone9(n0, n1);
    #pragma unroll
    for (int s = 0; s < 4; s++) { u32x4 o; for (int j = 0; j < 4; j++) o[j] = pkh(n0[4*s + j], n1[4*s + j]) | 0x10000u; *(u32x4*)(smd + lra + s*4096) = o; }
    asm volatile(WV(15) "\n s_waitcnt lgkmcnt(0)\n s_barrier" ::: "memory");
    #pragma unroll
    for (int i = 0; i < 4; i++) d[i] = *(const u32x4*)(smd + lwa + i*1024);
    if (cb == 0 || cb == 160) {
      const bool sp0 = (((lane & 3) ^ ((lane >> 4) & 3)) & 2) == 0;
      #pragma unroll
      for (int i = 0; i < 4; i++)
        #pragma unroll
        for (int j = 0; j < 4; j++) { u32 av = asu((ash(d[i][j]) + ash(dppu<0xB1>(d[i][j]))) * (_Float16)0.5f) | 0x10000u; d[i][j] = sp0 ? av : d[i][j]; }
    }
    #pragma unroll
    for (int i = 0; i < 4; i++) BST4(d[i], t1o, WS_T2 + i*16*TROW*16);
    RB(15); RB(16); RB(17);
  } else {
    const bool g2on = rr_ >= 21 && rr_ < 31;
    const u32 g2i = xc*10 + rr_ - 21;
    if (w < 2 && g2on) {
      const u16* wp = wm + 1280 + g2i*32; const u32 vo = ((lane >> 2) + 16*w)*7680 + (lane & 3)*16;
      #pragma unroll
      for (u32 i = 0; i < 20; i++) { const u32 ii = (rr_ & 1) ? (i + 10) % 20 : i; GDMA((2*ii + w)*1024, vo + ii*245760, wp); }
      XQPREP();
      asm volatile(WV(0) ::: "memory");
    } else if (w >= 2 || blk == 161) {
      const u32 job = w >= 2 ? blk : 160;
      TILEDMA(job + 0*160 >= 160 ? job : 160);
      XQPREP();
      asm volatile(WV(0) ::: "memory");
      TILEJOB(job >= 160 ? job : 160);
      if (w >= 2) H2FILL();
    } else XQPREP();
    if (g2on) {
      asm volatile("s_barrier" ::: "memory");
      float ac[8] = {0.f, 0.f, 0.f, 0.f, 0.f, 0.f, 0.f, 0.f};
      #pragma unroll
      for (int i = 0; i < 10; i++) { u32x4 hv = *(const u32x4*)(smd + i*4096 + t16); float s = sil[(t >> 2) + 64*i];
        #pragma unroll
        for (int j = 0; j < 4; j++) { ac[2*j] += s*bflo(hv[j]); ac[2*j+1] += s*bfhi(hv[j]); } }
      float v = red8(ac, lane);
      v += dpp<0x114>(v);
      float* red = (float*)(scr + 32768);
      if (lane & 4) red[w*32 + (lane & 3)*8 + (lane >> 3)] = v;
      LBAR();
      if (t < 16) {
        float v0 = red[2*t] + red[32 + 2*t] + red[64 + 2*t] + red[96 + 2*t], v1 = red[2*t + 1] + red[33 + 2*t] + red[65 + 2*t] + red[97 + 2*t];
        u32 p = pkbf(v0, v1);
        if (g2i >= 40 && g2i < 60) p = pkbf(1.f + bflo(p), 1.f + bfhi(p));
        GST2(p, tag, WS_GM + (640 + g2i*16 + t)*8);
      }
    }
    u32x4 ga_;
    asm volatile("buffer_load_dwordx4 %0,%1,%2,%3 offen sc1\n s_waitcnt vmcnt(0)\n s_barrier" : "=&v"(ga_) : "v"(lane < 48 ? l16 : 0u), "s"(rs), "s"((u32)WS_GW) : "memory");
    if (t == 192) GST2(0, tag, WS_GD + blk*8);
    NOUNROLL for (int sp = 0; sp < SPINMAX; sp++) {
      if (__all(ga_.y == tag && ga_.w == tag)) break;
      asm volatile("buffer_load_dwordx4 %0,%1,%2,%3 offen sc1\n s_waitcnt vmcnt(0)" : "=&v"(ga_) : "v"(lane < 48 ? l16 : 0u), "s"(rs), "s"((u32)WS_GW) : "memory");
    }
    eglob = (int)wmaxu(umax(ga_.x, ga_.z));
    if (w == 0) {
      NOUNROLL for (int sp = 0; sp < SPINMAX; sp++) {
        u32x2 g; asm volatile("buffer_load_dwordx2 %0,%1,%2,%3 offen sc1\n s_waitcnt vmcnt(0)" : "=&v"(g) : "v"(hx*8), "s"(rs), "s"((u32)WS_GD) : "memory");
        if (__builtin_amdgcn_readfirstlane(g.y) == tag) break;
        __builtin_amdgcn_s_sleep(10);
      }
    }
    asm volatile("s_barrier" ::: "memory");
    const u32 tc = (u32)__builtin_amdgcn_s_memrealtime();
    #pragma unroll
    for (int i = 0; i < 18; i++) RB(i);
    if (w < 2) while ((u32)__builtin_amdgcn_s_memrealtime() - tc < CANDELAY) __builtin_amdgcn_s_sleep(1);
  }
  const u32 pm2 = 2*pm + (xc & 1), pn2 = ((xc >> 1) & 1)*4 + (rr_ >> 3);
  const u32 dsrc = WS_WT2 + (pn2*240 + 60*w)*1024, dlds = 81920 + w*20480;
  const u32 s8 = (ROT & 1) ? rr_ & 7 : 0, s4 = (ROT & 4) ? rr_ >> 3 : 0;
  const u32 ksh = 2*((5*s4 + 1) >> 1);
  const u32 re_ = (ROT & 2) ? ((rr_ & 7) + 2*(rr_ >> 3)) & 7 : 0, rl_ = (ROT & 2) ? ((rr_ & 7) + (rr_ >> 3)) & 3 : 0;
#define WRAP(v, m) ((v) >= (m) ? (v) - (m) : (v))
#define KE(p) (3*(((p) + re_) & 3) + ((((p) + re_) >> 2) & 1))
#define KL(i) (3*(((i) + rl_) & 3) + 2)
#define AG(g) (WRAP((g) + (ksh >> 1), 10u)*4096)
#define P1S(g) (wtb + WRAP((g) + 2*ksh, 40u)*4096)
#define HG(i) WRAP(w*(RPER/4) + (i) + ((s8*RPER) >> 3), (u32)RPER)
#define HL(i) (HG(i)*4096)
#define HS(i) P1S(HG(i))
#define TG(i) (30 + WRAP(WRAP(3*w + (i), 10u) + s8, 10u))
#define TL(i) (TG(i)*4096)
#define TS(i) P1S(TG(i))
#define RG(i) WRAP(5*w + (i) + ((5*s8) >> 1), 20u)
#define RL(i) (RG(i)*4096)
#define RS(i) (wtb + 163840 + WRAP(RG(i) + ksh, 20u)*4096)
  u32x4 WD[40], xs0, xs1, xs2;
#define X1ST(i) BST4((i) == 0 ? xs0 : ((i) == 1 ? xs1 : xs2), (i) < 2 || t >= 128 ? t16 : t16 + 8192, WS_X1 + a*10240 + ((i) == 1 ? 4096 : 0))
  {
    const u32 n1 = lane & 31, h = lane >> 5;
    f32x4* m2 = (f32x4*)(scr + 22528);
    if (w < 2) {
      u32 sc[6], tof1;
      #pragma unroll
      for (int i = 0; i < 6; i++) { u32 k = (t >> 1) + 64*i; if (t & 1) k = 640 - k; const u32 q = k / 20; sc[i] = SCR + ((k - 20*q)*36 + q)*4; }
      if (t == 1) sc[0] = SCR + 128; if (t != 0) sc[5] = SCR + 132;
      tof1 = t == 0 ? 10240 : t16;
      asm volatile("" : "+v"(sc[0]), "+v"(sc[1]), "+v"(sc[2]), "+v"(sc[3]), "+v"(sc[4]), "+v"(sc[5]), "+v"(tof1));
      const u32 t2b = WS_T2 + a*TROW*16;
      u32x4 c[6];
      asm volatile(WV(0) ::: "memory");
      NOUNROLL for (int sp = 0; sp < SPINMAX; sp++) {
        asm volatile("buffer_load_dwordx4 %0,%6,%8,%9 offen sc1\n buffer_load_dwordx4 %1,%6,%8,%9 offen offset:2048 sc1\n buffer_load_dwordx4 %2,%6,%8,%10 offen sc1\n"
                     "buffer_load_dwordx4 %3,%6,%8,%10 offen offset:2048 sc1\n buffer_load_dwordx4 %4,%6,%8,%11 offen sc1\n buffer_load_dwordx4 %5,%7,%8,%9 offen sc1\n s_waitcnt vmcnt(0)"
                     : "=&v"(c[0]), "=&v"(c[1]), "=&v"(c[2]), "=&v"(c[3]), "=&v"(c[4]), "=&v"(c[5]) : "v"(t16), "v"(tof1), "s"(rs), "s"(t2b), "s"(t2b + 4096), "s"(t2b + 8192) : "memory");
        if (__all((and4(c[0]) & and4(c[1]) & and4(c[2]) & and4(c[3]) & and4(c[4]) & and4(c[5]) & 0x10000u) != 0)) break;
        LSLEEP();
      }
      #pragma unroll
      for (int i = 0; i < 6; i++)
        #pragma unroll
        for (int j = 0; j < 4; j++) *(u32*)(smd + sc[i] + j*2880) = c[i][j];
    } else {
      u32x4 g0, g1, g2, g3, g4, gd0, gd1;
      const u32 u0 = t - 128;
      NOUNROLL for (int sp = 0; sp < SPINMAX; sp++) {
        asm volatile("buffer_load_dwordx4 %0,%7,%9,%10 offen sc1\n buffer_load_dwordx4 %1,%7,%9,%10 offen offset:2048 sc1\n buffer_load_dwordx4 %2,%7,%9,%11 offen sc1\n buffer_load_dwordx4 %3,%7,%9,%11 offen offset:2048 sc1\n"
                     "buffer_load_dwordx4 %4,%7,%9,%12 offen sc1\n buffer_load_dwordx4 %5,%8,%9,%13 offen sc1\n buffer_load_dwordx4 %6,%8,%9,%13 offen sc1\n s_waitcnt vmcnt(0)"
                     : "=&v"(g0), "=&v"(g1), "=&v"(g2), "=&v"(g3), "=&v"(g4), "=&v"(gd0), "=&v"(gd1) : "v"(u0*16), "v"(lane < 48 ? l16 : 0u), "s"(rs), "s"((u32)(WS_GM + 5120)), "s"((u32)(WS_GM + 9216)), "s"((u32)(WS_GM + 13312)), "s"((u32)(WS_GD + 1280)) : "memory");
        if (__all((g0.y ^ tag | g0.w ^ tag | g1.y ^ tag | g1.w ^ tag | g2.y ^ tag | g2.w ^ tag | g3.y ^ tag | g3.w ^ tag | g4.y ^ tag | g4.w ^ tag | gd0.y ^ tag | gd0.w ^ tag) == 0)) break;
        LSLEEP();
      }
      m2[u0] = f32x4{bflo(g0.x), bfhi(g0.x), bflo(g0.z), bfhi(g0.z)}; m2[u0 + 128] = f32x4{bflo(g1.x), bfhi(g1.x), bflo(g1.z), bfhi(g1.z)};
      m2[u0 + 256] = f32x4{bflo(g2.x), bfhi(g2.x), bflo(g2.z), bfhi(g2.z)}; m2[u0 + 384] = f32x4{bflo(g3.x), bfhi(g3.x), bflo(g3.z), bfhi(g3.z)};
      #pragma unroll
      for (int j = 0; j < 4; j++) { const u32 o_ = 16384 + (w - 2)*16384 + j*4096; DMAS(o_, wtb + o_); DMAC(wtb + o_, 1024); DMAC(wtb + o_, 2048); DMAC(wtb + o_, 3072); }
    }
    f32x2 xv[10];
    #pragma unroll
    for (int i = 0; i < 10; i++) { xv[i] = f32x2{bflo(xq[i]), bfhi(xq[i])}; asm volatile("" : "+v"(xv[i])); }
    u32 hwa = SCR + 11520 + (2*w + (lane & 1))*1296 + (n1 >> 1)*4, hra = SCR + 11520 + (t & 7)*1296 + (t >> 3)*16, xwa = SCR + w*2880 + n1*4;
    u32 xr0 = SCR + (t / 160)*2880 + (t % 160)*16, xr1 = SCR + ((t + 256) / 160)*2880 + ((t + 256) % 160)*16, xr2 = t < 128 ? SCR + 3*2880 + (t + 32)*16 : xr0;
    float inv = pow2i(eglob - 1000) * __builtin_amdgcn_rcpf(s1sc);
    asm volatile("" : "+v"(hwa), "+v"(hra), "+v"(xwa), "+v"(xr0), "+v"(xr1), "+v"(xr2), "+v"(inv));
    LBAR();
    float ga[10], sb[10], gb[10];
    f32x16 o0, o1;
#define GASCALE() _Pragma("unroll") for (int i = 0; i < 10; i++) { ga[i] *= inv; asm volatile("" : "+v"(ga[i])); }
#define LDMOD() _Pragma("unroll") for (int i = 0; i < 10; i++) { const float* mp = (const float*)m2 + n1 + 32*K2MAP(i, h); ga[i] = mp[0]; sb[i] = mp[640]; gb[i] = mp[1280]; }
    FFT640(fra, o0, o1, PFC0(); LDMOD(), PFC1(), PFC2(), PFC3(), PFC4(); PFC5(); GASCALE(), PFC6(), PFC7(), PFC8(); PFC9());
    f32x2 sum = {0.f, 0.f}, sq = {0.f, 0.f};
    #pragma unroll
    for (int i = 0; i < 10; i++) {
      xv[i] = f32x2{o1[i]*ga[i] + xv[i].x, o0[i]*ga[i] + xv[i].y};
      *(u32*)(smd + xwa + 128*K2MAP(i, h)) = pkbf(xv[i].x, xv[i].y); sum += xv[i]; sq += xv[i]*xv[i];
      if (i == 1) { asm volatile("" : "+v"(sum)); PFC10(); }
      if (i == 3) { asm volatile("" : "+v"(sum)); PFC11(); }
      if (i == 5) { asm volatile("" : "+v"(sum)); PFC12(); }
      if (i == 7) { asm volatile("" : "+v"(sum)); PFC13(); }
    }
    asm volatile("" : "+v"(sum), "+v"(sq)); PFC14(); PFC15();
    float r4 = sw16(sw32(sum.x, sq.x), sw32(sum.y, sq.y));
    r4 += dpp<0x121>(r4); r4 += dpp<0x122>(r4); r4 += dpp<0x124>(r4); r4 += dpp<0x128>(r4);
    asm volatile("" : "+v"(r4)); PFC16(); PFC17();
    const f32x2 mean = f32x2{__builtin_bit_cast(float, __builtin_amdgcn_readlane(__builtin_bit_cast(int, r4), 0)), __builtin_bit_cast(float, __builtin_amdgcn_readlane(__builtin_bit_cast(int, r4), 16))} * (1.f/640.f);
    const f32x2 ssq = {__builtin_bit_cast(float, __builtin_amdgcn_readlane(__builtin_bit_cast(int, r4), 32)), __builtin_bit_cast(float, __builtin_amdgcn_readlane(__builtin_bit_cast(int, r4), 48))};
    f32x2 rd = ssq - mean*mean*640.f;
    rd = f32x2{lnrd(fmaxf(rd.x, 0.f)), lnrd(fmaxf(rd.y, 0.f))};
    asm volatile("" : "+v"(rd)); PFC18(); PFC19();
    {
      const u32 sel = (lane & 1) ? 0x03020706u : 0x05040100u;
      const f32x2 mr = mean*rd;
      #pragma unroll
      for (int i = 0; i < 10; i++) {
        f32x2 hv = (xv[i]*rd - mr)*gb[i] + sb[i];
        u32 p = pkbf(hv.x, hv.y);
        *(u32*)(smd + hwa + 64*K2MAP(i, h)) = __builtin_amdgcn_perm(dppu<0xB1>(p), p, sel);
        if (i == 1) PFC20();
        if (i == 3) PFC21();
        if (i == 4) PFC22();
        if (i == 6) PFC23();
        if (i == 7) PFC24();
        if (i == 8) PFC25();
      }
    }
    LBAR();
    {
      u32x4 e0 = *(const u32x4*)(smd + hra), e1 = *(const u32x4*)(smd + hra + 512), e2 = *(const u32x4*)(smd + hra + (t < 128 ? 1024 : 0));
      u32x4 f0 = *(const u32x4*)(smd + xr0), f1 = *(const u32x4*)(smd + xr1), f2 = *(const u32x4*)(smd + xr2);
      BST4(e0, h2o, h2b); BST4(e1, h2o, h2b + 16384); BST4(e2, t < 128 ? h2o + 32768 : h2o, h2b);
      xs0 = f0; xs1 = f1; xs2 = f2;
    }
    if (blk == 0 && t == 0) asm volatile("buffer_store_dword %0,off,%1,0 sc1" :: "v"(tag), "s"(rs) : "memory");
    asm volatile(WV(30) "\n s_barrier" ::: "memory");
  DMASO(TL(0), TS(0), 0);
  DMAC(TS(0), 1024);
  DMAC(TS(0), 2048);
  DMAC(TS(0), 3072);
  DMASO(TL(1), TS(1), 0);
  DMAC(TS(1), 1024);
  DMAC(TS(1), 2048);
  DMAC(TS(1), 3072);
  DMASO(TL(2), TS(2), 0);
  DMAC(TS(2), 1024);
  DMAC(TS(2), 2048);
  DMAC(TS(2), 3072);
  WLOAD(WD[22], KE(4)*5120 + 0, 2048);
  WLOAD(WD[23], KE(4)*5120 + 0, 3072);
  WLOAD(WD[24], KE(4)*5120 + 4096, 0);
  WLOAD(WD[25], KE(5)*5120 + 0, 0);

    asm volatile("s_waitcnt vmcnt(%0)\n s_barrier" :: "i"(NPF2) : "memory");
  }
  {
    const u32 asrc = WS_H2 + (pm*4 + w)*40960;
    const u32 rd0 = l16, rd1 = l16 + 61440, rd2 = l16 + 122880;
    const u32 xsrc = WS_HT + (pm2*48 + 12*w)*4096;
    u32 xvo[4];
    #pragma unroll
    for (u32 i = 0; i < 4; i++) xvo[i] = l16 + ((w + i) & 3)*1024;
    const u32 rds = dlds + l16;
    const u32 rho = pm2*64 + w*16 + (lane & 15), n0 = pn2*80 + 20*(lane >> 4);
    const u32 x1b = WS_X1 + ((pm2*32 + w*8)*640 + pn2*80)*4, gbb = WS_GM + (1600 + pn2*40)*8;
    u32 edo[4];
    #pragma unroll
    for (u32 k = 0; k < 3; k++) { const u32 u = k < 2 || lane < 32 ? 64*k + lane : lane; edo[k] = (u / 20)*2560 + (u % 20)*16; }
    edo[3] = (lane < 20 ? lane : 0)*16;
    u32 exa = w*4096 + ((lane & 15) >> 1)*320 + (lane >> 4)*80, ega = w*4096 + 3072 + (lane >> 4)*80;
    asm volatile("" : "+v"(edo[0]), "+v"(edo[1]), "+v"(edo[2]), "+v"(edo[3]), "+v"(exa), "+v"(ega), "+v"(xvo[0]), "+v"(xvo[1]), "+v"(xvo[2]), "+v"(xvo[3]));
    u32x4 A[40], W0, W1, W2, W3, W4, W5, X[48], V[20], EX[5], EG[5];
    f32x4 C[12][2], Y[4][5]; float ea, eb;
    u32 mchk, eo[4]; u32x2 gf;
#define GFLOAD(CON) asm volatile("buffer_load_dwordx2 %0,%1,%2,%3 offen sc1" : CON(gf) : "v"((lane & 15)*8), "s"(rs), "s"(WS_GF + pm*128) : "memory")
  ALOAD(A[0], AG(0), 0);
  ALOAD(A[1], AG(0), 1024);
  ALOAD(A[2], AG(0), 2048);
  ALOAD(A[3], AG(0), 3072);
  ALOAD(A[4], AG(1), 0);
  ALOAD(A[5], AG(1), 1024);
  ALOAD(A[6], AG(1), 2048);
  ALOAD(A[7], AG(1), 3072);
  ALOAD(A[8], AG(2), 0);
  ALOAD(A[9], AG(2), 1024);
  ALOAD(A[10], AG(2), 2048);
  ALOAD(A[11], AG(2), 3072);
  WLOAD(WD[26], KE(5)*5120 + 0, 1024);
  WLOAD(WD[27], KE(5)*5120 + 0, 2048);
  WLOAD(WD[28], KE(5)*5120 + 0, 3072);
  WLOAD(WD[29], KE(5)*5120 + 4096, 0);
  WLOAD(WD[30], KE(6)*5120 + 0, 0);
  WLOAD(WD[31], KE(6)*5120 + 0, 1024);
  WLOAD(WD[32], KE(6)*5120 + 0, 2048);
  WLOAD(WD[33], KE(6)*5120 + 0, 3072);
  asm volatile("s_waitcnt vmcnt(16)" : "+v"(A[0]),"+v"(A[1]),"+v"(A[2]),"+v"(A[3]));
  if (__any(max4m(max4m(max4m(max4(A[0]),A[1]),A[2]),A[3]) == SENT)) { NOUNROLL for (int sp = 0; sp < SPINMAX; sp++) {
    asm volatile("s_waitcnt vmcnt(0)" ::: "memory");
    ARELOAD(A[0], AOFF(0));
    ARELOAD(A[1], AOFF(1));
    ARELOAD(A[2], AOFF(2));
    ARELOAD(A[3], AOFF(3));
    ARELOAD(A[4], AOFF(4));
    ARELOAD(A[5], AOFF(5));
    ARELOAD(A[6], AOFF(6));
    ARELOAD(A[7], AOFF(7));
    ARELOAD(A[8], AOFF(8));
    ARELOAD(A[9], AOFF(9));
    ARELOAD(A[10], AOFF(10));
    ARELOAD(A[11], AOFF(11));
    asm volatile("s_waitcnt vmcnt(0)" : "+v"(A[0]),"+v"(A[1]),"+v"(A[2]),"+v"(A[3]));
    if (!__any(max4m(max4m(max4m(max4(A[0]),A[1]),A[2]),A[3]) == SENT)) break; } }
  LRD(W0, rd0, 0);
  LRD(W1, rd0, 1024);
  LRD(W2, rd0, 2048);
  LRD(W3, rd0, 3072);
  LRD(W4, rd0, 4096);
  LRD(W5, rd0, 5120);
  MAZ("s_waitcnt lgkmcnt(4)\n", C[0][0], W0, A[0]);
  CKA(15, A[4], 1);
  MBZ(C[0][1], W0, A[1], rd0, 6144);
  MAZ("", C[1][0], W1, A[0]);
  ALOAD(A[12], AG(3), 0);
  MBZ(C[1][1], W1, A[1], rd0, 7168);
  MAZ("s_waitcnt lgkmcnt(4)\n", C[2][0], W2, A[0]);
  CKB(A[4]);
  MBZ(C[2][1], W2, A[1], rd0, 8192);
  MAZ("", C[3][0], W3, A[0]);
  X1ST(0);
  MBZ(C[3][1], W3, A[1], rd0, 9216);
  MAZ("s_waitcnt lgkmcnt(4)\n", C[4][0], W4, A[0]);
  CKA(16, A[5], 0);
  MBZ(C[4][1], W4, A[1], rd0, 10240);
  MAZ("", C[5][0], W5, A[0]);
  ALOAD(A[13], AG(3), 1024);
  MBZ(C[5][1], W5, A[1], rd0, 11264);
  MAZ("s_waitcnt lgkmcnt(4)\n", C[6][0], W0, A[0]);
  CKB(A[5]);
  MBZ(C[6][1], W0, A[1], rd0, 12288);
  MAZ("", C[7][0], W1, A[0]);
  X1ST(1);
  MBZ(C[7][1], W1, A[1], rd0, 13312);
  MA("s_waitcnt lgkmcnt(4)\n", C[0][0], W2, A[2]);
  CKA(17, A[6], 0);
  MB(C[0][1], W2, A[3], rd0, 14336);
  MA("", C[1][0], W3, A[2]);
  ALOAD(A[14], AG(3), 2048);
  MB(C[1][1], W3, A[3], rd0, 15360);
  MA("s_waitcnt lgkmcnt(4)\n", C[2][0], W4, A[2]);
  CKB(A[6]);
  MB(C[2][1], W4, A[3], rd0, 16384);
  MA("", C[3][0], W5, A[2]);
  X1ST(2);
  MB(C[3][1], W5, A[3], rd0, 17408);
  MA("s_waitcnt lgkmcnt(4)\n", C[4][0], W0, A[2]);
  CKA(18, A[7], 0);
  MB(C[4][1], W0, A[3], rd0, 18432);
  MA("", C[5][0], W1, A[2]);
  ALOAD(A[15], AG(3), 3072);
  MB(C[5][1], W1, A[3], rd0, 19456);
  MA("s_waitcnt lgkmcnt(4)\n", C[6][0], W2, A[2]);
  CKB(A[7]);
  MB(C[6][1], W2, A[3], rd0, 20480);
  CHECK(1);
  MA("", C[7][0], W3, A[2]);
  HTF(0);
  MB(C[7][1], W3, A[3], rd0, 21504);
  MA("s_waitcnt lgkmcnt(4)\n", C[0][0], W4, A[4]);
  CKA(19, A[8], 1);
  MB(C[0][1], W4, A[5], rd0, 22528);
  MA("", C[1][0], W5, A[4]);
  ALOAD(A[16], AG(4), 0);
  MB(C[1][1], W5, A[5], rd0, 23552);
  MA("s_waitcnt lgkmcnt(4)\n", C[2][0], W0, A[4]);
  CKB(A[8]);
  MB(C[2][1], W0, A[5], rd0, 24576);
  MA("", C[3][0], W1, A[4]);
  HTF(1);
  MB(C[3][1], W1, A[5], rd0, 25600);
  MA("s_waitcnt lgkmcnt(4)\n", C[4][0], W2, A[4]);
  CKA(20, A[9], 0);
  MB(C[4][1], W2, A[5], rd0, 26624);
  MA("", C[5][0], W3, A[4]);
  ALOAD(A[17], AG(4), 1024);
  MB(C[5][1], W3, A[5], rd0, 27648);
  MA("s_waitcnt lgkmcnt(4)\n", C[6][0], W4, A[4]);
  CKB(A[9]);
  MB(C[6][1], W4, A[5], rd0, 28672);
  MA("", C[7][0], W5, A[4]);
  HTF(2);
  MB(C[7][1], W5, A[5], rd0, 29696);
  MA("s_waitcnt lgkmcnt(4)\n", C[0][0], W0, A[6]);
  CKA(21, A[10], 0);
  MB(C[0][1], W0, A[7], rd0, 30720);
  MA("", C[1][0], W1, A[6]);
  ALOAD(A[18], AG(4), 2048);
  MB(C[1][1], W1, A[7], rd0, 31744);
  MA("s_waitcnt lgkmcnt(4)\n", C[2][0], W2, A[6]);
  CKB(A[10]);
  MB(C[2][1], W2, A[7], rd0, 32768);
  MA("", C[3][0], W3, A[6]);
  HTF(3);
  MB(C[3][1], W3, A[7], rd0, 33792);
  MA("s_waitcnt lgkmcnt(4)\n", C[4][0], W4, A[6]);
  CKA(22, A[11], 0);
  MB(C[4][1], W4, A[7], rd0, 34816);
  MA("", C[5][0], W5, A[6]);
  ALOAD(A[19], AG(4), 3072);
  MB(C[5][1], W5, A[7], rd0, 35840);
  MA("s_waitcnt lgkmcnt(4)\n", C[6][0], W0, A[6]);
  CKB(A[11]);
  MB(C[6][1], W0, A[7], rd0, 36864);
  CHECK(2);
  MA("", C[7][0], W1, A[6]);
  HTF(4);
  MB(C[7][1], W1, A[7], rd0, 37888);
  MA("s_waitcnt lgkmcnt(4)\n", C[0][0], W2, A[8]);
  CKA(15, A[12], 1);
  MB(C[0][1], W2, A[9], rd0, 38912);
  MA("", C[1][0], W3, A[8]);
  ALOAD(A[20], AG(5), 0);
  MB(C[1][1], W3, A[9], rd0, 39936);
  MA("s_waitcnt lgkmcnt(4)\n", C[2][0], W4, A[8]);
  CKB(A[12]);
  MB(C[2][1], W4, A[9], rd0, 40960);
  MA("", C[3][0], W5, A[8]);
  HTF(5);
  MB(C[3][1], W5, A[9], rd0, 41984);
  MA("s_waitcnt lgkmcnt(4)\n", C[4][0], W0, A[8]);
  CKA(15, A[13], 0);
  MB(C[4][1], W0, A[9], rd0, 43008);
  MA("", C[5][0], W1, A[8]);
  ALOAD(A[21], AG(5), 1024);
  MB(C[5][1], W1, A[9], rd0, 44032);
  MA("s_waitcnt lgkmcnt(4)\n", C[6][0], W2, A[8]);
  CKB(A[13]);
  MB(C[6][1], W2, A[9], rd0, 45056);
  MA("", C[7][0], W3, A[8]);
  MB(C[7][1], W3, A[9], rd0, 46080);
  MA("s_waitcnt lgkmcnt(4)\n", C[0][0], W4, A[10]);
  CKA(14, A[14], 0);
  MB(C[0][1], W4, A[11], rd0, 47104);
  MA("", C[1][0], W5, A[10]);
  ALOAD(A[22], AG(5), 2048);
  MB(C[1][1], W5, A[11], rd0, 48128);
  MA("s_waitcnt lgkmcnt(4)\n", C[2][0], W0, A[10]);
  CKB(A[14]);
  MB(C[2][1], W0, A[11], rd0, 49152);
  MA("", C[3][0], W1, A[10]);
  MB(C[3][1], W1, A[11], rd0, 50176);
  MA("s_waitcnt lgkmcnt(4)\n", C[4][0], W2, A[10]);
  CKA(13, A[15], 0);
  MB(C[4][1], W2, A[11], rd0, 51200);
  MA("", C[5][0], W3, A[10]);
  ALOAD(A[23], AG(5), 3072);
  MB(C[5][1], W3, A[11], rd0, 52224);
  MA("s_waitcnt lgkmcnt(4)\n", C[6][0], W4, A[10]);
  CKB(A[15]);
  MB(C[6][1], W4, A[11], rd0, 53248);
  CHECK(3);
  MA("", C[7][0], W5, A[10]);
  MB(C[7][1], W5, A[11], rd0, 54272);
  MA("s_waitcnt lgkmcnt(4)\n", C[0][0], W0, A[12]);
  CKA(12, A[16], 1);
  MB(C[0][1], W0, A[13], rd0, 55296);
  MA("", C[1][0], W1, A[12]);
  ALOAD(A[24], AG(6), 0);
  MB(C[1][1], W1, A[13], rd0, 56320);
  MA("s_waitcnt lgkmcnt(4)\n", C[2][0], W2, A[12]);
  CKB(A[16]);
  MB(C[2][1], W2, A[13], rd0, 57344);
  MA("", C[3][0], W3, A[12]);
  MB(C[3][1], W3, A[13], rd0, 58368);
  MA("s_waitcnt lgkmcnt(4)\n", C[4][0], W4, A[12]);
  CKA(11, A[17], 0);
  MB(C[4][1], W4, A[13], rd0, 59392);
  MA("", C[5][0], W5, A[12]);
  ALOAD(A[25], AG(6), 1024);
  MB(C[5][1], W5, A[13], rd0, 60416);
  MA("s_waitcnt lgkmcnt(4)\n", C[6][0], W0, A[12]);
  CKB(A[17]);
  MB(C[6][1], W0, A[13], rd1, 0);
  MA("", C[7][0], W1, A[12]);
  MB(C[7][1], W1, A[13], rd1, 1024);
  MA("s_waitcnt lgkmcnt(4)\n", C[0][0], W2, A[14]);
  CKA(10, A[18], 0);
  MB(C[0][1], W2, A[15], rd1, 2048);
  MA("", C[1][0], W3, A[14]);
  ALOAD(A[26], AG(6), 2048);
  MB(C[1][1], W3, A[15], rd1, 3072);
  MA("s_waitcnt lgkmcnt(4)\n", C[2][0], W4, A[14]);
  CKB(A[18]);
  MB(C[2][1], W4, A[15], rd1, 4096);
  MA("", C[3][0], W5, A[14]);
  MB(C[3][1], W5, A[15], rd1, 5120);
  MA("s_waitcnt lgkmcnt(4)\n", C[4][0], W0, A[14]);
  CKA(9, A[19], 0);
  MB(C[4][1], W0, A[15], rd1, 6144);
  MA("", C[5][0], W1, A[14]);
  ALOAD(A[27], AG(6), 3072);
  MB(C[5][1], W1, A[15], rd1, 7168);
  MA("s_waitcnt lgkmcnt(4)\n", C[6][0], W2, A[14]);
  CKB(A[19]);
  MB(C[6][1], W2, A[15], rd1, 8192);
  CHECK(4);
  MA("", C[7][0], W3, A[14]);
  MB(C[7][1], W3, A[15], rd1, 9216);
  MA("s_waitcnt lgkmcnt(4)\n", C[0][0], W4, A[16]);
  CKA(8, A[20], 1);
  MB(C[0][1], W4, A[17], rd1, 10240);
  MA("", C[1][0], W5, A[16]);
  ALOAD(A[28], AG(7), 0);
  MB(C[1][1], W5, A[17], rd1, 11264);
  MA("s_waitcnt lgkmcnt(4)\n", C[2][0], W0, A[16]);
  CKB(A[20]);
  MB(C[2][1], W0, A[17], rd1, 12288);
  MA("", C[3][0], W1, A[16]);
  MB(C[3][1], W1, A[17], rd1, 13312);
  MA("s_waitcnt lgkmcnt(4)\n", C[4][0], W2, A[16]);
  CKA(7, A[21], 0);
  MB(C[4][1], W2, A[17], rd1, 14336);
  MA("", C[5][0], W3, A[16]);
  ALOAD(A[29], AG(7), 1024);
  MB(C[5][1], W3, A[17], rd1, 15360);
  MA("s_waitcnt lgkmcnt(4)\n", C[6][0], W4, A[16]);
  CKB(A[21]);
  MB(C[6][1], W4, A[17], rd1, 16384);
  MA("", C[7][0], W5, A[16]);
  MB(C[7][1], W5, A[17], rd1, 17408);
  MA("s_waitcnt lgkmcnt(4)\n", C[0][0], W0, A[18]);
  CKA(7, A[22], 0);
  MB(C[0][1], W0, A[19], rd1, 18432);
  MA("", C[1][0], W1, A[18]);
  ALOAD(A[30], AG(7), 2048);
  MB(C[1][1], W1, A[19], rd1, 19456);
  MA("s_waitcnt lgkmcnt(4)\n", C[2][0], W2, A[18]);
  CKB(A[22]);
  MB(C[2][1], W2, A[19], rd1, 20480);
  MA("", C[3][0], W3, A[18]);
  MB(C[3][1], W3, A[19], rd1, 21504);
  MA("s_waitcnt lgkmcnt(4)\n", C[4][0], W4, A[18]);
  CKA(7, A[23], 0);
  MB(C[4][1], W4, A[19], rd1, 22528);
  MA("", C[5][0], W5, A[18]);
  ALOAD(A[31], AG(7), 3072);
  MB(C[5][1], W5, A[19], rd1, 23552);
  MA("s_waitcnt lgkmcnt(4)\n", C[6][0], W0, A[18]);
  CKB(A[23]);
  MB(C[6][1], W0, A[19], rd1, 24576);
  CHECK(5);
  MA("", C[7][0], W1, A[18]);
  MB(C[7][1], W1, A[19], rd1, 25600);
  MA("s_waitcnt lgkmcnt(4)\n", C[0][0], W2, A[20]);
  CKA(7, A[24], 1);
  MB(C[0][1], W2, A[21], rd1, 26624);
  MA("", C[1][0], W3, A[20]);
  ALOAD(A[32], AG(8), 0);
  MB(C[1][1], W3, A[21], rd1, 27648);
  MA("s_waitcnt lgkmcnt(4)\n", C[2][0], W4, A[20]);
  CKB(A[24]);
  MB(C[2][1], W4, A[21], rd1, 28672);
  MA("", C[3][0], W5, A[20]);
  MB(C[3][1], W5, A[21], rd1, 29696);
  MA("s_waitcnt lgkmcnt(4)\n", C[4][0], W0, A[20]);
  CKA(7, A[25], 0);
  MB(C[4][1], W0, A[21], rd1, 30720);
  MA("", C[5][0], W1, A[20]);
  ALOAD(A[33], AG(8), 1024);
  MB(C[5][1], W1, A[21], rd1, 31744);
  asm volatile("s_waitcnt vmcnt(13)\n s_barrier" ::: "memory");
  GFPUB();
  MA("s_waitcnt lgkmcnt(4)\n", C[6][0], W2, A[20]);
  CKB(A[25]);
  MB(C[6][1], W2, A[21], rd1, 32768);
  MA("", C[7][0], W3, A[20]);
  DMASO(RL(0), RS(0), 0);
  MB(C[7][1], W3, A[21], rd1, 33792);
  MA("s_waitcnt lgkmcnt(4)\n", C[0][0], W4, A[22]);
  CKA(8, A[26], 0);
  MB(C[0][1], W4, A[23], rd1, 34816);
  MA("", C[1][0], W5, A[22]);
  ALOAD(A[34], AG(8), 2048);
  MB(C[1][1], W5, A[23], rd1, 35840);
  MA("s_waitcnt lgkmcnt(4)\n", C[2][0], W0, A[22]);
  CKB(A[26]);
  MB(C[2][1], W0, A[23], rd1, 36864);
  MA("", C[3][0], W1, A[22]);
  DMAC(RS(0), 1024);
  MB(C[3][1], W1, A[23], rd1, 37888);
  MA("s_waitcnt lgkmcnt(4)\n", C[4][0], W2, A[22]);
  CKA(9, A[27], 0);
  MB(C[4][1], W2, A[23], rd1, 38912);
  MA("", C[5][0], W3, A[22]);
  ALOAD(A[35], AG(8), 3072);
  MB(C[5][1], W3, A[23], rd1, 39936);
  MA("s_waitcnt lgkmcnt(4)\n", C[6][0], W4, A[22]);
  CKB(A[27]);
  MB(C[6][1], W4, A[23], rd1, 40960);
  CHECK(6);
  MA("", C[7][0], W5, A[22]);
  DMAC(RS(0), 2048);
  MB(C[7][1], W5, A[23], rd1, 41984);
  MA("s_waitcnt lgkmcnt(4)\n", C[0][0], W0, A[24]);
  CKA(10, A[28], 1);
  MB(C[0][1], W0, A[25], rd1, 43008);
  MA("", C[1][0], W1, A[24]);
  ALOAD(A[36], AG(9), 0);
  MB(C[1][1], W1, A[25], rd1, 44032);
  MA("s_waitcnt lgkmcnt(4)\n", C[2][0], W2, A[24]);
  CKB(A[28]);
  MB(C[2][1], W2, A[25], rd1, 45056);
  MA("", C[3][0], W3, A[24]);
  DMAC(RS(0), 3072);
  MB(C[3][1], W3, A[25], rd1, 46080);
  MA("s_waitcnt lgkmcnt(4)\n", C[4][0], W4, A[24]);
  CKA(11, A[29], 0);
  MB(C[4][1], W4, A[25], rd1, 47104);
  MA("", C[5][0], W5, A[24]);
  ALOAD(A[37], AG(9), 1024);
  MB(C[5][1], W5, A[25], rd1, 48128);
  MA("s_waitcnt lgkmcnt(4)\n", C[6][0], W0, A[24]);
  CKB(A[29]);
  MB(C[6][1], W0, A[25], rd1, 49152);
  MA("", C[7][0], W1, A[24]);
  DMASO(RL(1), RS(1), 0);
  MB(C[7][1], W1, A[25], rd1, 50176);
  MA("s_waitcnt lgkmcnt(4)\n", C[0][0], W2, A[26]);
  CKA(12, A[30], 0);
  MB(C[0][1], W2, A[27], rd1, 51200);
  MA("", C[1][0], W3, A[26]);
  ALOAD(A[38], AG(9), 2048);
  MB(C[1][1], W3, A[27], rd1, 52224);
  MA("s_waitcnt lgkmcnt(4)\n", C[2][0], W4, A[26]);
  CKB(A[30]);
  MB(C[2][1], W4, A[27], rd1, 53248);
  MA("", C[3][0], W5, A[26]);
  DMAC(RS(1), 1024);
  MB(C[3][1], W5, A[27], rd1, 54272);
  MA("s_waitcnt lgkmcnt(4)\n", C[4][0], W0, A[26]);
  CKA(13, A[31], 0);
  MB(C[4][1], W0, A[27], rd1, 55296);
  MA("", C[5][0], W1, A[26]);
  ALOAD(A[39], AG(9), 3072);
  MB(C[5][1], W1, A[27], rd1, 56320);
  MA("s_waitcnt lgkmcnt(4)\n", C[6][0], W2, A[26]);
  CKB(A[31]);
  MB(C[6][1], W2, A[27], rd1, 57344);
  CHECK(7);
  MA("", C[7][0], W3, A[26]);
  DMAC(RS(1), 2048);
  MB(C[7][1], W3, A[27], rd1, 58368);
  MA("s_waitcnt lgkmcnt(4)\n", C[0][0], W4, A[28]);
  CKA(14, A[32], 1);
  MB(C[0][1], W4, A[29], rd1, 59392);
  MA("", C[1][0], W5, A[28]);
  DMAC(RS(1), 3072);
  MB(C[1][1], W5, A[29], rd1, 60416);
  MA("s_waitcnt lgkmcnt(4)\n", C[2][0], W0, A[28]);
  CKB(A[32]);
  MB(C[2][1], W0, A[29], rd2, 0);
  MA("", C[3][0], W1, A[28]);
  DMASO(RL(2), RS(2), 0);
  MB(C[3][1], W1, A[29], rd2, 1024);
  MA("s_waitcnt lgkmcnt(4)\n", C[4][0], W2, A[28]);
  CKA(15, A[33], 0);
  MB(C[4][1], W2, A[29], rd2, 2048);
  MA("", C[5][0], W3, A[28]);
  DMAC(RS(2), 1024);
  MB(C[5][1], W3, A[29], rd2, 3072);
  MA("s_waitcnt lgkmcnt(4)\n", C[6][0], W4, A[28]);
  CKB(A[33]);
  MB(C[6][1], W4, A[29], rd2, 4096);
  MA("", C[7][0], W5, A[28]);
  DMAC(RS(2), 2048);
  MB(C[7][1], W5, A[29], rd2, 5120);
  MA("s_waitcnt lgkmcnt(4)\n", C[0][0], W0, A[30]);
  CKA(15, A[34], 0);
  MB(C[0][1], W0, A[31], rd2, 6144);
  MA("", C[1][0], W1, A[30]);
  DMAC(RS(2), 3072);
  MB(C[1][1], W1, A[31], rd2, 7168);
  MA("s_waitcnt lgkmcnt(4)\n", C[2][0], W2, A[30]);
  CKB(A[34]);
  MB(C[2][1], W2, A[31], rd2, 8192);
  MA("", C[3][0], W3, A[30]);
  DMASO(RL(3), RS(3), 0);
  MB(C[3][1], W3, A[31], rd2, 9216);
  MA("s_waitcnt lgkmcnt(4)\n", C[4][0], W4, A[30]);
  CKA(15, A[35], 0);
  MB(C[4][1], W4, A[31], rd2, 10240);
  MA("", C[5][0], W5, A[30]);
  DMAC(RS(3), 1024);
  MB(C[5][1], W5, A[31], rd2, 11264);
  MA("s_waitcnt lgkmcnt(4)\n", C[6][0], W0, A[30]);
  CKB(A[35]);
  MB(C[6][1], W0, A[31], rd2, 12288);
  CHECK(8);
  MA("", C[7][0], W1, A[30]);
  DMAC(RS(3), 2048);
  MB(C[7][1], W1, A[31], rd2, 13312);
  MA("s_waitcnt lgkmcnt(4)\n", C[0][0], W2, A[32]);
  CKA(15, A[36], 1);
  MB(C[0][1], W2, A[33], rd2, 14336);
  MA("", C[1][0], W3, A[32]);
  DMAC(RS(3), 3072);
  MB(C[1][1], W3, A[33], rd2, 15360);
  MA("s_waitcnt lgkmcnt(4)\n", C[2][0], W4, A[32]);
  CKB(A[36]);
  MB(C[2][1], W4, A[33], rd2, 16384);
  MA("", C[3][0], W5, A[32]);
  DMASO(RL(4), RS(4), 0);
  MB(C[3][1], W5, A[33], rd2, 17408);
  MA("s_waitcnt lgkmcnt(4)\n", C[4][0], W0, A[32]);
  CKA(15, A[37], 0);
  MB(C[4][1], W0, A[33], rd2, 18432);
  MA("", C[5][0], W1, A[32]);
  DMAC(RS(4), 1024);
  MB(C[5][1], W1, A[33], rd2, 19456);
  MA("s_waitcnt lgkmcnt(4)\n", C[6][0], W2, A[32]);
  CKB(A[37]);
  MB(C[6][1], W2, A[33], rd2, 20480);
  MA("", C[7][0], W3, A[32]);
  DMAC(RS(4), 2048);
  MB(C[7][1], W3, A[33], rd2, 21504);
  MA("s_waitcnt lgkmcnt(4)\n", C[0][0], W4, A[34]);
  CKA(15, A[38], 0);
  MB(C[0][1], W4, A[35], rd2, 22528);
  MA("", C[1][0], W5, A[34]);
  DMAC(RS(4), 3072);
  MB(C[1][1], W5, A[35], rd2, 23552);
  MA("s_waitcnt lgkmcnt(4)\n", C[2][0], W0, A[34]);
  CKB(A[38]);
  MB(C[2][1], W0, A[35], rd2, 24576);
  MA("", C[3][0], W1, A[34]);
  MB(C[3][1], W1, A[35], rd2, 25600);
  MA("s_waitcnt lgkmcnt(4)\n", C[4][0], W2, A[34]);
  CKA(14, A[39], 0);
  MB(C[4][1], W2, A[35], rd2, 26624);
  MA("", C[5][0], W3, A[34]);
  MB(C[5][1], W3, A[35], rd2, 27648);
  MA("s_waitcnt lgkmcnt(4)\n", C[6][0], W4, A[34]);
  CKB(A[39]);
  MB(C[6][1], W4, A[35], rd2, 28672);
  CHECK(9);
  MA("", C[7][0], W5, A[34]);
  MB(C[7][1], W5, A[35], rd2, 29696);
  MA("s_waitcnt lgkmcnt(4)\n", C[0][0], W0, A[36]);
  MB(C[0][1], W0, A[37], rd2, 30720);
  MA("", C[1][0], W1, A[36]);
  MB(C[1][1], W1, A[37], rd2, 31744);
  MA("s_waitcnt lgkmcnt(4)\n", C[2][0], W2, A[36]);
  MB(C[2][1], W2, A[37], rd2, 32768);
  MA("", C[3][0], W3, A[36]);
  MB(C[3][1], W3, A[37], rd2, 33792);
  MA("s_waitcnt lgkmcnt(4)\n", C[4][0], W4, A[36]);
  MB(C[4][1], W4, A[37], rd2, 34816);
  MA("", C[5][0], W5, A[36]);
  MB(C[5][1], W5, A[37], rd2, 35840);
  MA("s_waitcnt lgkmcnt(4)\n", C[6][0], W0, A[36]);
  MB(C[6][1], W0, A[37], rd2, 36864);
  MA("", C[7][0], W1, A[36]);
  MB(C[7][1], W1, A[37], rd2, 37888);
  MA("s_waitcnt lgkmcnt(4)\n", C[0][0], W2, A[38]);
  MB(C[0][1], W2, A[39], rd2, 38912);
  MA("", C[1][0], W3, A[38]);
  WLOAD(WD[34], KE(6)*5120 + 4096, 0);
  MB(C[1][1], W3, A[39], rd2, 39936);
  asm volatile("s_waitcnt vmcnt(1)\n s_barrier" ::: "memory");
  MA("s_waitcnt lgkmcnt(4)\n", C[2][0], W4, A[38]);
  MB(C[2][1], W4, A[39], rd0, 0);
  MA("", C[3][0], W5, A[38]);
  WLOADV(WD[35], KE(7)*5120 + 0, 0);
  MB(C[3][1], W5, A[39], rd0, 1024);
  MA("s_waitcnt lgkmcnt(4)\n", C[4][0], W0, A[38]);
  MB(C[4][1], W0, A[39], rd0, 2048);
  MA("", C[5][0], W1, A[38]);
  WLOADV(WD[36], KE(7)*5120 + 0, 1024);
  MB(C[5][1], W1, A[39], rd0, 3072);
  MA("s_waitcnt lgkmcnt(4)\n", C[6][0], W2, A[38]);
  MB(C[6][1], W2, A[39], rd0, 4096);
  MA("", C[7][0], W3, A[38]);
  WLOADV(WD[37], KE(7)*5120 + 0, 2048);
  MB(C[7][1], W3, A[39], rd0, 5120);
  MAZ("s_waitcnt lgkmcnt(4)\n", C[8][0], W4, A[0]);
  MBZ(C[8][1], W4, A[1], rd0, 6144);
  MAZ("", C[9][0], W5, A[0]);
  WLOADV(WD[38], KE(7)*5120 + 0, 3072);
  MBZ(C[9][1], W5, A[1], rd0, 7168);
  MAZ("s_waitcnt lgkmcnt(4)\n", C[10][0], W0, A[0]);
  MBZ(C[10][1], W0, A[1], rd0, 8192);
  MAZ("", C[11][0], W1, A[0]);
  WLOADV(WD[39], KE(7)*5120 + 4096, 0);
  MBZ(C[11][1], W1, A[1], rd0, 9216);
  MA("s_waitcnt lgkmcnt(4)\n", C[8][0], W2, A[2]);
  EPA(C[0][0], C[2][0], 0, ea);
  MB(C[8][1], W2, A[3], rd0, 10240);
  EPA(C[0][0], C[2][0], 1, eb);
  MA("", C[9][0], W3, A[2]);
  EPC(0);
  MB(C[9][1], W3, A[3], rd0, 11264);
  EPA(C[0][0], C[2][0], 2, ea);
  asm volatile("s_barrier" ::: "memory");
  MA("s_waitcnt lgkmcnt(4)\n", C[10][0], W4, A[2]);
  EPA(C[0][0], C[2][0], 3, eb);
  MB(C[10][1], W4, A[3], rd0, 12288);
  EPC(1);
  MA("", C[11][0], W5, A[2]);
  EPA(C[1][0], C[3][0], 0, ea);
  MB(C[11][1], W5, A[3], rd0, 13312);
  EPA(C[1][0], C[3][0], 1, eb);
  MA("s_waitcnt lgkmcnt(4)\n", C[8][0], W0, A[4]);
  EPC(2);
  MB(C[8][1], W0, A[5], rd0, 14336);
  EPA(C[1][0], C[3][0], 2, ea);
  MA("", C[9][0], W1, A[4]);
  EPA(C[1][0], C[3][0], 3, eb);
  MB(C[9][1], W1, A[5], rd0, 15360);
  EPC(3);
  MA("s_waitcnt lgkmcnt(4)\n", C[10][0], W2, A[4]);
  EPIS(0);
  MB(C[10][1], W2, A[5], rd0, 16384);
  EPA(C[0][1], C[2][1], 0, ea);
  MA("", C[11][0], W3, A[4]);
  EPA(C[0][1], C[2][1], 1, eb);
  MB(C[11][1], W3, A[5], rd0, 17408);
  EPC(0);
  MA("s_waitcnt lgkmcnt(4)\n", C[8][0], W4, A[6]);
  EPA(C[0][1], C[2][1], 2, ea);
  MB(C[8][1], W4, A[7], rd0, 18432);
  EPA(C[0][1], C[2][1], 3, eb);
  MA("", C[9][0], W5, A[6]);
  EPC(1);
  MB(C[9][1], W5, A[7], rd0, 19456);
  EPA(C[1][1], C[3][1], 0, ea);
  MA("s_waitcnt lgkmcnt(4)\n", C[10][0], W0, A[6]);
  EPA(C[1][1], C[3][1], 1, eb);
  MB(C[10][1], W0, A[7], rd0, 20480);
  EPC(2);
  MA("", C[11][0], W1, A[6]);
  EPA(C[1][1], C[3][1], 2, ea);
  MB(C[11][1], W1, A[7], rd0, 21504);
  EPA(C[1][1], C[3][1], 3, eb);
  MA("s_waitcnt lgkmcnt(4)\n", C[8][0], W2, A[8]);
  EPC(3);
  MB(C[8][1], W2, A[9], rd0, 22528);
  EPIS(1);
  MA("", C[9][0], W3, A[8]);
  EPA(C[4][0], C[6][0], 0, ea);
  MB(C[9][1], W3, A[9], rd0, 23552);
  EPA(C[4][0], C[6][0], 1, eb);
  MA("s_waitcnt lgkmcnt(4)\n", C[10][0], W4, A[8]);
  EPC(0);
  MB(C[10][1], W4, A[9], rd0, 24576);
  EPA(C[4][0], C[6][0], 2, ea);
  MA("", C[11][0], W5, A[8]);
  EPA(C[4][0], C[6][0], 3, eb);
  MB(C[11][1], W5, A[9], rd0, 25600);
  EPC(1);
  MA("s_waitcnt lgkmcnt(4)\n", C[8][0], W0, A[10]);
  EPA(C[5][0], C[7][0], 0, ea);
  MB(C[8][1], W0, A[11], rd0, 26624);
  EPA(C[5][0], C[7][0], 1, eb);
  MA("", C[9][0], W1, A[10]);
  EPC(2);
  MB(C[9][1], W1, A[11], rd0, 27648);
  EPA(C[5][0], C[7][0], 2, ea);
  MA("s_waitcnt lgkmcnt(4)\n", C[10][0], W2, A[10]);
  EPA(C[5][0], C[7][0], 3, eb);
  MB(C[10][1], W2, A[11], rd0, 28672);
  EPC(3);
  MA("", C[11][0], W3, A[10]);
  EPIS(2);
  MB(C[11][1], W3, A[11], rd0, 29696);
  EPA(C[4][1], C[6][1], 0, ea);
  MA("s_waitcnt lgkmcnt(4)\n", C[8][0], W4, A[12]);
  EPA(C[4][1], C[6][1], 1, eb);
  MB(C[8][1], W4, A[13], rd0, 30720);
  EPC(0);
  MA("", C[9][0], W5, A[12]);
  EPA(C[4][1], C[6][1], 2, ea);
  MB(C[9][1], W5, A[13], rd0, 31744);
  EPA(C[4][1], C[6][1], 3, eb);
  MA("s_waitcnt lgkmcnt(4)\n", C[10][0], W0, A[12]);
  EPC(1);
  MB(C[10][1], W0, A[13], rd0, 32768);
  EPA(C[5][1], C[7][1], 0, ea);
  MA("", C[11][0], W1, A[12]);
  EPA(C[5][1], C[7][1], 1, eb);
  MB(C[11][1], W1, A[13], rd0, 33792);
  EPC(2);
  MA("s_waitcnt lgkmcnt(4)\n", C[8][0], W2, A[14]);
  EPA(C[5][1], C[7][1], 2, ea);
  MB(C[8][1], W2, A[15], rd0, 34816);
  EPA(C[5][1], C[7][1], 3, eb);
  MA("", C[9][0], W3, A[14]);
  EPC(3);
  MB(C[9][1], W3, A[15], rd0, 35840);
  EPIS(3);
  MA("s_waitcnt lgkmcnt(4)\n", C[10][0], W4, A[14]);
  DMASO(dlds + 0, dsrc + KL(0)*5120, 0);
  GFLOAD("=v");
  MB(C[10][1], W4, A[15], rd0, 36864);
  MA("", C[11][0], W5, A[14]);
  DMAC(dsrc + KL(0)*5120, 1024);
  MB(C[11][1], W5, A[15], rd0, 37888);
  MA("s_waitcnt lgkmcnt(4)\n", C[8][0], W0, A[16]);
  DMAC(dsrc + KL(0)*5120, 2048);
  MB(C[8][1], W0, A[17], rd0, 38912);
  MA("", C[9][0], W1, A[16]);
  DMAC(dsrc + KL(0)*5120, 3072);
  MB(C[9][1], W1, A[17], rd0, 39936);
  MA("s_waitcnt lgkmcnt(4)\n", C[10][0], W2, A[16]);
  DMAS(dlds + 4096, dsrc + KL(0)*5120 + 4096);
  MB(C[10][1], W2, A[17], rd0, 40960);
  MA("", C[11][0], W3, A[16]);
  DMASO(dlds + 5120, dsrc + KL(1)*5120, 0);
  MB(C[11][1], W3, A[17], rd0, 41984);
  MA("s_waitcnt lgkmcnt(4)\n", C[8][0], W4, A[18]);
  DMAC(dsrc + KL(1)*5120, 1024);
  MB(C[8][1], W4, A[19], rd0, 43008);
  MA("", C[9][0], W5, A[18]);
  DMAC(dsrc + KL(1)*5120, 2048);
  MB(C[9][1], W5, A[19], rd0, 44032);
  MA("s_waitcnt lgkmcnt(4)\n", C[10][0], W0, A[18]);
  DMAC(dsrc + KL(1)*5120, 3072);
  MB(C[10][1], W0, A[19], rd0, 45056);
  MA("", C[11][0], W1, A[18]);
  DMAS(dlds + 9216, dsrc + KL(1)*5120 + 4096);
  MB(C[11][1], W1, A[19], rd0, 46080);
  MA("s_waitcnt lgkmcnt(4)\n", C[8][0], W2, A[20]);
  MB(C[8][1], W2, A[21], rd0, 47104);
  MA("", C[9][0], W3, A[20]);
  DMASO(dlds + 10240, dsrc + KL(2)*5120, 0);
  MB(C[9][1], W3, A[21], rd0, 48128);
  MA("s_waitcnt lgkmcnt(4)\n", C[10][0], W4, A[20]);
  DMAC(dsrc + KL(2)*5120, 1024);
  MB(C[10][1], W4, A[21], rd0, 49152);
  MA("", C[11][0], W5, A[20]);
  DMAC(dsrc + KL(2)*5120, 2048);
  MB(C[11][1], W5, A[21], rd0, 50176);
  MA("s_waitcnt lgkmcnt(4)\n", C[8][0], W0, A[22]);
  DMAC(dsrc + KL(2)*5120, 3072);
  MB(C[8][1], W0, A[23], rd0, 51200);
  MA("", C[9][0], W1, A[22]);
  DMAS(dlds + 14336, dsrc + KL(2)*5120 + 4096);
  MB(C[9][1], W1, A[23], rd0, 52224);
  MA("s_waitcnt lgkmcnt(4)\n", C[10][0], W2, A[22]);
  DMASO(dlds + 15360, dsrc + KL(3)*5120, 0);
  MB(C[10][1], W2, A[23], rd0, 53248);
  MA("", C[11][0], W3, A[22]);
  DMAC(dsrc + KL(3)*5120, 1024);
  MB(C[11][1], W3, A[23], rd0, 54272);
  MA("s_waitcnt lgkmcnt(4)\n", C[8][0], W4, A[24]);
  DMAC(dsrc + KL(3)*5120, 2048);
  MB(C[8][1], W4, A[25], rd0, 55296);
  MA("", C[9][0], W5, A[24]);
  DMAC(dsrc + KL(3)*5120, 3072);
  MB(C[9][1], W5, A[25], rd0, 56320);
  MA("s_waitcnt lgkmcnt(4)\n", C[10][0], W0, A[24]);
  DMAS(dlds + 19456, dsrc + KL(3)*5120 + 4096);
  MB(C[10][1], W0, A[25], rd0, 57344);
  MA("", C[11][0], W1, A[24]);
  MB(C[11][1], W1, A[25], rd0, 58368);
  MA("s_waitcnt lgkmcnt(4)\n", C[8][0], W2, A[26]);
  MB(C[8][1], W2, A[27], rd0, 59392);
  asm volatile("s_waitcnt vmcnt(19)" : "+v"(gf));
  if (__builtin_expect(!__all(gf.y == tag), 0)) { NOUNROLL for (int sp = 0; sp < SPINMAX; sp++) { GFLOAD("+v"); asm volatile("s_waitcnt vmcnt(0)" : "+v"(gf)); if (__all(gf.y == tag)) break; } }
  MA("", C[9][0], W3, A[26]);
  MB(C[9][1], W3, A[27], rd0, 60416);
  MA("s_waitcnt lgkmcnt(4)\n", C[10][0], W4, A[26]);
  XLOAD(X[0], 0, KE(0)*4096);
  MB(C[10][1], W4, A[27], rd1, 0);
  MA("", C[11][0], W5, A[26]);
  MB(C[11][1], W5, A[27], rd1, 1024);
  MA("s_waitcnt lgkmcnt(4)\n", C[8][0], W0, A[28]);
  XLOAD(X[1], 1, KE(0)*4096);
  MB(C[8][1], W0, A[29], rd1, 2048);
  MA("", C[9][0], W1, A[28]);
  MB(C[9][1], W1, A[29], rd1, 3072);
  MA("s_waitcnt lgkmcnt(4)\n", C[10][0], W2, A[28]);
  XLOAD(X[2], 2, KE(0)*4096);
  MB(C[10][1], W2, A[29], rd1, 4096);
  MA("", C[11][0], W3, A[28]);
  MB(C[11][1], W3, A[29], rd1, 5120);
  MA("s_waitcnt lgkmcnt(4)\n", C[8][0], W4, A[30]);
  XLOAD(X[3], 3, KE(0)*4096);
  MB(C[8][1], W4, A[31], rd1, 6144);
  MA("", C[9][0], W5, A[30]);
  MB(C[9][1], W5, A[31], rd1, 7168);
  MA("s_waitcnt lgkmcnt(4)\n", C[10][0], W0, A[30]);
  XLOAD(X[4], 0, KE(1)*4096);
  MB(C[10][1], W0, A[31], rd1, 8192);
  MA("", C[11][0], W1, A[30]);
  MB(C[11][1], W1, A[31], rd1, 9216);
  MA("s_waitcnt lgkmcnt(4)\n", C[8][0], W2, A[32]);
  XLOAD(X[5], 1, KE(1)*4096);
  MB(C[8][1], W2, A[33], rd1, 10240);
  MA("", C[9][0], W3, A[32]);
  MB(C[9][1], W3, A[33], rd1, 11264);
  MA("s_waitcnt lgkmcnt(4)\n", C[10][0], W4, A[32]);
  XLOAD(X[6], 2, KE(1)*4096);
  MB(C[10][1], W4, A[33], rd1, 12288);
  MA("", C[11][0], W5, A[32]);
  MB(C[11][1], W5, A[33], rd1, 13312);
  MA("s_waitcnt lgkmcnt(4)\n", C[8][0], W0, A[34]);
  XLOAD(X[7], 3, KE(1)*4096);
  MB(C[8][1], W0, A[35], rd1, 14336);
  MA("", C[9][0], W1, A[34]);
  MB(C[9][1], W1, A[35], rd1, 15360);
  MA("s_waitcnt lgkmcnt(4)\n", C[10][0], W2, A[34]);
  XLOAD(X[8], 0, KE(2)*4096);
  MB(C[10][1], W2, A[35], rd1, 16384);
  MA("", C[11][0], W3, A[34]);
  MB(C[11][1], W3, A[35], rd1, 17408);
  MA("s_waitcnt lgkmcnt(4)\n", C[8][0], W4, A[36]);
  XLOAD(X[9], 1, KE(2)*4096);
  MB(C[8][1], W4, A[37], rd1, 18432);
  MA("", C[9][0], W5, A[36]);
  MB(C[9][1], W5, A[37], rd1, 19456);
  MA("s_waitcnt lgkmcnt(4)\n", C[10][0], W0, A[36]);
  XLOAD(X[10], 2, KE(2)*4096);
  MA("", C[10][1], W0, A[37]);
  MA("", C[11][0], W1, A[36]);
  MA("", C[11][1], W1, A[37]);
  MA("s_waitcnt lgkmcnt(2)\n", C[8][0], W2, A[38]);
  XLOAD(X[11], 3, KE(2)*4096);
  MA("", C[8][1], W2, A[39]);
  MA("", C[9][0], W3, A[38]);
  MA("", C[9][1], W3, A[39]);
  MA("s_waitcnt lgkmcnt(0)\n", C[10][0], W4, A[38]);
  MA("", C[10][1], W4, A[39]);
  MA("", C[11][0], W5, A[38]);
  MA("", C[11][1], W5, A[39]);
  CKA(11, X[0], 1);
  CKB(X[0]);
  CKA(10, X[1], 0);
  CKB(X[1]);
  CKA(9, X[2], 0);
  CKB(X[2]);
  CKA(8, X[3], 0);
  CKB(X[3]);
  if (__builtin_expect(__any(mchk == SENT), 0)) { asm volatile("s_waitcnt vmcnt(0)" ::: "memory"); NOUNROLL for (int sp = 0; sp < SPINMAX; sp++) {
  XRELOAD(X[0], 0, KE(0)*4096);
  XRELOAD(X[1], 1, KE(0)*4096);
  XRELOAD(X[2], 2, KE(0)*4096);
  XRELOAD(X[3], 3, KE(0)*4096);
  XRELOAD(X[4], 0, KE(1)*4096);
  XRELOAD(X[5], 1, KE(1)*4096);
  XRELOAD(X[6], 2, KE(1)*4096);
  XRELOAD(X[7], 3, KE(1)*4096);
  XRELOAD(X[8], 0, KE(2)*4096);
  XRELOAD(X[9], 1, KE(2)*4096);
  XRELOAD(X[10], 2, KE(2)*4096);
  XRELOAD(X[11], 3, KE(2)*4096);
    asm volatile("s_waitcnt vmcnt(0)" : "+v"(X[0]),"+v"(X[1]),"+v"(X[2]),"+v"(X[3]));
    if (!__any(max4m(max4m(max4m(max4(X[0]),X[1]),X[2]),X[3]) == SENT)) break; } }
  MGZ(Y[0][0], WD[0], X[0]);
  XLOAD(X[12], 0, KE(3)*4096);
  MGZ(Y[1][0], WD[0], X[1]);
  EPA(C[8][0], C[10][0], 0, ea);
  MGZ(Y[2][0], WD[0], X[2]);
  EPA(C[8][0], C[10][0], 1, eb);
  MGZ(Y[3][0], WD[0], X[3]);
  EPC(0);
  MGZ(Y[0][1], WD[1], X[0]);
  EPA(C[8][0], C[10][0], 2, ea);
  XLOAD(X[13], 1, KE(3)*4096);
  MGZ(Y[1][1], WD[1], X[1]);
  EPA(C[8][0], C[10][0], 3, eb);
  CKA(9, X[4], 1);
  MGZ(Y[2][1], WD[1], X[2]);
  EPC(1);
  MGZ(Y[3][1], WD[1], X[3]);
  EPA(C[9][0], C[11][0], 0, ea);
  CKB(X[4]);
  MGZ(Y[0][2], WD[2], X[0]);
  EPA(C[9][0], C[11][0], 1, eb);
  XLOAD(X[14], 2, KE(3)*4096);
  MGZ(Y[1][2], WD[2], X[1]);
  EPC(2);
  CKA(9, X[5], 0);
  MGZ(Y[2][2], WD[2], X[2]);
  EPA(C[9][0], C[11][0], 2, ea);
  MGZ(Y[3][2], WD[2], X[3]);
  EPA(C[9][0], C[11][0], 3, eb);
  CKB(X[5]);
  MGZ(Y[0][3], WD[3], X[0]);
  EPC(3);
  XLOAD(X[15], 3, KE(3)*4096);
  MGZ(Y[1][3], WD[3], X[1]);
  EPIS(4);
  CKA(10, X[6], 0);
  MGZ(Y[2][3], WD[3], X[2]);
  EPA(C[8][1], C[10][1], 0, ea);
  MGZ(Y[3][3], WD[3], X[3]);
  EPA(C[8][1], C[10][1], 1, eb);
  CKB(X[6]);
  MGZ(Y[0][4], WD[4], X[0]);
  EPC(0);
  XLOAD(X[16], 0, KE(4)*4096);
  MGZ(Y[1][4], WD[4], X[1]);
  EPA(C[8][1], C[10][1], 2, ea);
  CKA(10, X[7], 0);
  MGZ(Y[2][4], WD[4], X[2]);
  EPA(C[8][1], C[10][1], 3, eb);
  MGZ(Y[3][4], WD[4], X[3]);
  EPC(1);
  CKB(X[7]);
  if (__builtin_expect(__any(mchk == SENT), 0)) { asm volatile("s_waitcnt vmcnt(0)" ::: "memory"); NOUNROLL for (int sp = 0; sp < SPINMAX; sp++) {
  XRELOAD(X[4], 0, KE(1)*4096);
  XRELOAD(X[5], 1, KE(1)*4096);
  XRELOAD(X[6], 2, KE(1)*4096);
  XRELOAD(X[7], 3, KE(1)*4096);
  XRELOAD(X[8], 0, KE(2)*4096);
  XRELOAD(X[9], 1, KE(2)*4096);
  XRELOAD(X[10], 2, KE(2)*4096);
  XRELOAD(X[11], 3, KE(2)*4096);
  XRELOAD(X[12], 0, KE(3)*4096);
  XRELOAD(X[13], 1, KE(3)*4096);
  XRELOAD(X[14], 2, KE(3)*4096);
  XRELOAD(X[15], 3, KE(3)*4096);
    asm volatile("s_waitcnt vmcnt(0)" : "+v"(X[4]),"+v"(X[5]),"+v"(X[6]),"+v"(X[7]));
    if (!__any(max4m(max4m(max4m(max4(X[4]),X[5]),X[6]),X[7]) == SENT)) break; } }
  MG(Y[0][0], WD[5], X[4]);
  EPA(C[9][1], C[11][1], 0, ea);
  XLOAD(X[17], 1, KE(4)*4096);
  MG(Y[1][0], WD[5], X[5]);
  EPA(C[9][1], C[11][1], 1, eb);
  MG(Y[2][0], WD[5], X[6]);
  EPC(2);
  MG(Y[3][0], WD[5], X[7]);
  EPA(C[9][1], C[11][1], 2, ea);
  MG(Y[0][1], WD[6], X[4]);
  EPA(C[9][1], C[11][1], 3, eb);
  XLOAD(X[18], 2, KE(4)*4096);
  MG(Y[1][1], WD[6], X[5]);
  EPC(3);
  CKA(11, X[8], 1);
  MG(Y[2][1], WD[6], X[6]);
  EPIS(5);
  MG(Y[3][1], WD[6], X[7]);
  CKB(X[8]);
  MG(Y[0][2], WD[7], X[4]);
  XLOAD(X[19], 3, KE(4)*4096);
  MG(Y[1][2], WD[7], X[5]);
  CKA(12, X[9], 0);
  MG(Y[2][2], WD[7], X[6]);
  MG(Y[3][2], WD[7], X[7]);
  CKB(X[9]);
  MG(Y[0][3], WD[8], X[4]);
  XLOAD(X[20], 0, KE(5)*4096);
  MG(Y[1][3], WD[8], X[5]);
  CKA(12, X[10], 0);
  MG(Y[2][3], WD[8], X[6]);
  MG(Y[3][3], WD[8], X[7]);
  CKB(X[10]);
  MG(Y[0][4], WD[9], X[4]);
  XLOAD(X[21], 1, KE(5)*4096);
  MG(Y[1][4], WD[9], X[5]);
  CKA(12, X[11], 0);
  MG(Y[2][4], WD[9], X[6]);
  MG(Y[3][4], WD[9], X[7]);
  CKB(X[11]);
  if (__builtin_expect(__any(mchk == SENT), 0)) { asm volatile("s_waitcnt vmcnt(0)" ::: "memory"); NOUNROLL for (int sp = 0; sp < SPINMAX; sp++) {
  XRELOAD(X[8], 0, KE(2)*4096);
  XRELOAD(X[9], 1, KE(2)*4096);
  XRELOAD(X[10], 2, KE(2)*4096);
  XRELOAD(X[11], 3, KE(2)*4096);
  XRELOAD(X[12], 0, KE(3)*4096);
  XRELOAD(X[13], 1, KE(3)*4096);
  XRELOAD(X[14], 2, KE(3)*4096);
  XRELOAD(X[15], 3, KE(3)*4096);
  XRELOAD(X[16], 0, KE(4)*4096);
  XRELOAD(X[17], 1, KE(4)*4096);
  XRELOAD(X[18], 2, KE(4)*4096);
  XRELOAD(X[19], 3, KE(4)*4096);
    asm volatile("s_waitcnt vmcnt(0)" : "+v"(X[8]),"+v"(X[9]),"+v"(X[10]),"+v"(X[11]));
    if (!__any(max4m(max4m(max4m(max4(X[8]),X[9]),X[10]),X[11]) == SENT)) break; } }
  MG(Y[0][0], WD[10], X[8]);
  XLOAD(X[22], 2, KE(5)*4096);
  MG(Y[1][0], WD[10], X[9]);
  MG(Y[2][0], WD[10], X[10]);
  MG(Y[3][0], WD[10], X[11]);
  MG(Y[0][1], WD[11], X[8]);
  XLOAD(X[23], 3, KE(5)*4096);
  MG(Y[1][1], WD[11], X[9]);
  CKA(13, X[12], 1);
  MG(Y[2][1], WD[11], X[10]);
  MG(Y[3][1], WD[11], X[11]);
  CKB(X[12]);
  MG(Y[0][2], WD[12], X[8]);
  XLOAD(X[24], 0, KE(6)*4096);
  MG(Y[1][2], WD[12], X[9]);
  CKA(13, X[13], 0);
  MG(Y[2][2], WD[12], X[10]);
  MG(Y[3][2], WD[12], X[11]);
  CKB(X[13]);
  MG(Y[0][3], WD[13], X[8]);
  XLOAD(X[25], 1, KE(6)*4096);
  MG(Y[1][3], WD[13], X[9]);
  CKA(13, X[14], 0);
  MG(Y[2][3], WD[13], X[10]);
  MG(Y[3][3], WD[13], X[11]);
  CKB(X[14]);
  MG(Y[0][4], WD[14], X[8]);
  XLOAD(X[26], 2, KE(6)*4096);
  MG(Y[1][4], WD[14], X[9]);
  CKA(13, X[15], 0);
  MG(Y[2][4], WD[14], X[10]);
  MG(Y[3][4], WD[14], X[11]);
  CKB(X[15]);
  if (__builtin_expect(__any(mchk == SENT), 0)) { asm volatile("s_waitcnt vmcnt(0)" ::: "memory"); NOUNROLL for (int sp = 0; sp < SPINMAX; sp++) {
  XRELOAD(X[12], 0, KE(3)*4096);
  XRELOAD(X[13], 1, KE(3)*4096);
  XRELOAD(X[14], 2, KE(3)*4096);
  XRELOAD(X[15], 3, KE(3)*4096);
  XRELOAD(X[16], 0, KE(4)*4096);
  XRELOAD(X[17], 1, KE(4)*4096);
  XRELOAD(X[18], 2, KE(4)*4096);
  XRELOAD(X[19], 3, KE(4)*4096);
  XRELOAD(X[20], 0, KE(5)*4096);
  XRELOAD(X[21], 1, KE(5)*4096);
  XRELOAD(X[22], 2, KE(5)*4096);
  XRELOAD(X[23], 3, KE(5)*4096);
    asm volatile("s_waitcnt vmcnt(0)" : "+v"(X[12]),"+v"(X[13]),"+v"(X[14]),"+v"(X[15]));
    if (!__any(max4m(max4m(max4m(max4(X[12]),X[13]),X[14]),X[15]) == SENT)) break; } }
  MG(Y[0][0], WD[15], X[12]);
  XLOAD(X[27], 3, KE(6)*4096);
  MG(Y[1][0], WD[15], X[13]);
  MG(Y[2][0], WD[15], X[14]);
  MG(Y[3][0], WD[15], X[15]);
  MG(Y[0][1], WD[16], X[12]);
  XLOAD(X[28], 0, KE(7)*4096);
  MG(Y[1][1], WD[16], X[13]);
  CKA(13, X[16], 1);
  MG(Y[2][1], WD[16], X[14]);
  MG(Y[3][1], WD[16], X[15]);
  CKB(X[16]);
  MG(Y[0][2], WD[17], X[12]);
  XLOAD(X[29], 1, KE(7)*4096);
  MG(Y[1][2], WD[17], X[13]);
  CKA(13, X[17], 0);
  MG(Y[2][2], WD[17], X[14]);
  MG(Y[3][2], WD[17], X[15]);
  CKB(X[17]);
  MG(Y[0][3], WD[18], X[12]);
  XLOAD(X[30], 2, KE(7)*4096);
  MG(Y[1][3], WD[18], X[13]);
  CKA(13, X[18], 0);
  MG(Y[2][3], WD[18], X[14]);
  MG(Y[3][3], WD[18], X[15]);
  CKB(X[18]);
  MG(Y[0][4], WD[19], X[12]);
  XLOAD(X[31], 3, KE(7)*4096);
  MG(Y[1][4], WD[19], X[13]);
  CKA(12, X[19], 0);
  MG(Y[2][4], WD[19], X[14]);
  MG(Y[3][4], WD[19], X[15]);
  CKB(X[19]);
  if (__builtin_expect(__any(mchk == SENT), 0)) { asm volatile("s_waitcnt vmcnt(0)" ::: "memory"); NOUNROLL for (int sp = 0; sp < SPINMAX; sp++) {
  XRELOAD(X[16], 0, KE(4)*4096);
  XRELOAD(X[17], 1, KE(4)*4096);
  XRELOAD(X[18], 2, KE(4)*4096);
  XRELOAD(X[19], 3, KE(4)*4096);
  XRELOAD(X[20], 0, KE(5)*4096);
  XRELOAD(X[21], 1, KE(5)*4096);
  XRELOAD(X[22], 2, KE(5)*4096);
  XRELOAD(X[23], 3, KE(5)*4096);
  XRELOAD(X[24], 0, KE(6)*4096);
  XRELOAD(X[25], 1, KE(6)*4096);
  XRELOAD(X[26], 2, KE(6)*4096);
  XRELOAD(X[27], 3, KE(6)*4096);
  XRELOAD(X[28], 0, KE(7)*4096);
  XRELOAD(X[29], 1, KE(7)*4096);
  XRELOAD(X[30], 2, KE(7)*4096);
  XRELOAD(X[31], 3, KE(7)*4096);
    asm volatile("s_waitcnt vmcnt(0)" : "+v"(X[16]),"+v"(X[17]),"+v"(X[18]),"+v"(X[19]));
    if (!__any(max4m(max4m(max4m(max4(X[16]),X[17]),X[18]),X[19]) == SENT)) break; } }
  MG(Y[0][0], WD[20], X[16]);
  XLOAD(X[32], 0, KL(0)*4096);
  MG(Y[1][0], WD[20], X[17]);
  MG(Y[2][0], WD[20], X[18]);
  MG(Y[3][0], WD[20], X[19]);
  MG(Y[0][1], WD[21], X[16]);
  XLOAD(X[33], 1, KL(0)*4096);
  MG(Y[1][1], WD[21], X[17]);
  CKA(13, X[20], 1);
  MG(Y[2][1], WD[21], X[18]);
  MG(Y[3][1], WD[21], X[19]);
  CKB(X[20]);
  MG(Y[0][2], WD[22], X[16]);
  XLOAD(X[34], 2, KL(0)*4096);
  MG(Y[1][2], WD[22], X[17]);
  CKA(13, X[21], 0);
  MG(Y[2][2], WD[22], X[18]);
  MG(Y[3][2], WD[22], X[19]);
  CKB(X[21]);
  MG(Y[0][3], WD[23], X[16]);
  XLOAD(X[35], 3, KL(0)*4096);
  MG(Y[1][3], WD[23], X[17]);
  CKA(13, X[22], 0);
  MG(Y[2][3], WD[23], X[18]);
  MG(Y[3][3], WD[23], X[19]);
  CKB(X[22]);
  MG(Y[0][4], WD[24], X[16]);
  XLOAD(X[36], 0, KL(1)*4096);
  MG(Y[1][4], WD[24], X[17]);
  CKA(13, X[23], 0);
  MG(Y[2][4], WD[24], X[18]);
  MG(Y[3][4], WD[24], X[19]);
  CKB(X[23]);
  if (__builtin_expect(__any(mchk == SENT), 0)) { asm volatile("s_waitcnt vmcnt(0)" ::: "memory"); NOUNROLL for (int sp = 0; sp < SPINMAX; sp++) {
  XRELOAD(X[20], 0, KE(5)*4096);
  XRELOAD(X[21], 1, KE(5)*4096);
  XRELOAD(X[22], 2, KE(5)*4096);
  XRELOAD(X[23], 3, KE(5)*4096);
  XRELOAD(X[24], 0, KE(6)*4096);
  XRELOAD(X[25], 1, KE(6)*4096);
  XRELOAD(X[26], 2, KE(6)*4096);
  XRELOAD(X[27], 3, KE(6)*4096);
  XRELOAD(X[28], 0, KE(7)*4096);
  XRELOAD(X[29], 1, KE(7)*4096);
  XRELOAD(X[30], 2, KE(7)*4096);
  XRELOAD(X[31], 3, KE(7)*4096);
  XRELOAD(X[32], 0, KL(0)*4096);
  XRELOAD(X[33], 1, KL(0)*4096);
  XRELOAD(X[34], 2, KL(0)*4096);
  XRELOAD(X[35], 3, KL(0)*4096);
    asm volatile("s_waitcnt vmcnt(0)" : "+v"(X[20]),"+v"(X[21]),"+v"(X[22]),"+v"(X[23]));
    if (!__any(max4m(max4m(max4m(max4(X[20]),X[21]),X[22]),X[23]) == SENT)) break; } }
  MG(Y[0][0], WD[25], X[20]);
  XLOAD(X[37], 1, KL(1)*4096);
  MG(Y[1][0], WD[25], X[21]);
  MG(Y[2][0], WD[25], X[22]);
  MG(Y[3][0], WD[25], X[23]);
  MG(Y[0][1], WD[26], X[20]);
  XLOAD(X[38], 2, KL(1)*4096);
  MG(Y[1][1], WD[26], X[21]);
  CKA(14, X[24], 1);
  MG(Y[2][1], WD[26], X[22]);
  asm volatile("s_waitcnt vmcnt(41)" ::: "memory");
  LRDA(V[0], rds, 0);
  LRDA(V[1], rds, 1024);
  MG(Y[3][1], WD[26], X[23]);
  CKB(X[24]);
  MG(Y[0][2], WD[27], X[20]);
  XLOAD(X[39], 3, KL(1)*4096);
  MG(Y[1][2], WD[27], X[21]);
  CKA(14, X[25], 0);
  MG(Y[2][2], WD[27], X[22]);
  LRDA(V[2], rds, 2048);
  LRDA(V[3], rds, 3072);
  MG(Y[3][2], WD[27], X[23]);
  CKB(X[25]);
  MG(Y[0][3], WD[28], X[20]);
  XLOAD(X[40], 0, KL(2)*4096);
  MG(Y[1][3], WD[28], X[21]);
  CKA(14, X[26], 0);
  MG(Y[2][3], WD[28], X[22]);
  LRDA(V[4], rds, 4096);
  LRDA(V[5], rds, 5120);
  MG(Y[3][3], WD[28], X[23]);
  CKB(X[26]);
  MG(Y[0][4], WD[29], X[20]);
  XLOAD(X[41], 1, KL(2)*4096);
  MG(Y[1][4], WD[29], X[21]);
  CKA(14, X[27], 0);
  MG(Y[2][4], WD[29], X[22]);
  LRDA(V[6], rds, 6144);
  LRDA(V[7], rds, 7168);
  MG(Y[3][4], WD[29], X[23]);
  CKB(X[27]);
  if (__builtin_expect(__any(mchk == SENT), 0)) { asm volatile("s_waitcnt vmcnt(0)" ::: "memory"); NOUNROLL for (int sp = 0; sp < SPINMAX; sp++) {
  XRELOAD(X[24], 0, KE(6)*4096);
  XRELOAD(X[25], 1, KE(6)*4096);
  XRELOAD(X[26], 2, KE(6)*4096);
  XRELOAD(X[27], 3, KE(6)*4096);
  XRELOAD(X[28], 0, KE(7)*4096);
  XRELOAD(X[29], 1, KE(7)*4096);
  XRELOAD(X[30], 2, KE(7)*4096);
  XRELOAD(X[31], 3, KE(7)*4096);
  XRELOAD(X[32], 0, KL(0)*4096);
  XRELOAD(X[33], 1, KL(0)*4096);
  XRELOAD(X[34], 2, KL(0)*4096);
  XRELOAD(X[35], 3, KL(0)*4096);
  XRELOAD(X[36], 0, KL(1)*4096);
  XRELOAD(X[37], 1, KL(1)*4096);
  XRELOAD(X[38], 2, KL(1)*4096);
  XRELOAD(X[39], 3, KL(1)*4096);
    asm volatile("s_waitcnt vmcnt(0)" : "+v"(X[24]),"+v"(X[25]),"+v"(X[26]),"+v"(X[27]));
    if (!__any(max4m(max4m(max4m(max4(X[24]),X[25]),X[26]),X[27]) == SENT)) break; } }
  MG(Y[0][0], WD[30], X[24]);
  XLOAD(X[42], 2, KL(2)*4096);
  MG(Y[1][0], WD[30], X[25]);
  MG(Y[2][0], WD[30], X[26]);
  LRDA(V[8], rds, 8192);
  LRDA(V[9], rds, 9216);
  MG(Y[3][0], WD[30], X[27]);
  MG(Y[0][1], WD[31], X[24]);
  XLOAD(X[43], 3, KL(2)*4096);
  MG(Y[1][1], WD[31], X[25]);
  CKA(15, X[28], 1);
  MG(Y[2][1], WD[31], X[26]);
  LRDA(V[10], rds, 10240);
  LRDA(V[11], rds, 11264);
  MG(Y[3][1], WD[31], X[27]);
  CKB(X[28]);
  MG(Y[0][2], WD[32], X[24]);
  XLOAD(X[44], 0, KL(3)*4096);
  MG(Y[1][2], WD[32], X[25]);
  CKA(15, X[29], 0);
  MG(Y[2][2], WD[32], X[26]);
  LRDA(V[12], rds, 12288);
  MG(Y[3][2], WD[32], X[27]);
  CKB(X[29]);
  MG(Y[0][3], WD[33], X[24]);
  XLOAD(X[45], 1, KL(3)*4096);
  MG(Y[1][3], WD[33], X[25]);
  CKA(15, X[30], 0);
  MG(Y[2][3], WD[33], X[26]);
  LRDA(V[13], rds, 13312);
  MG(Y[3][3], WD[33], X[27]);
  CKB(X[30]);
  MG(Y[0][4], WD[34], X[24]);
  XLOAD(X[46], 2, KL(3)*4096);
  MG(Y[1][4], WD[34], X[25]);
  CKA(15, X[31], 0);
  MG(Y[2][4], WD[34], X[26]);
  LRDA(V[14], rds, 14336);
  MG(Y[3][4], WD[34], X[27]);
  CKB(X[31]);
  if (__builtin_expect(__any(mchk == SENT), 0)) { asm volatile("s_waitcnt vmcnt(0)" ::: "memory"); NOUNROLL for (int sp = 0; sp < SPINMAX; sp++) {
  XRELOAD(X[28], 0, KE(7)*4096);
  XRELOAD(X[29], 1, KE(7)*4096);
  XRELOAD(X[30], 2, KE(7)*4096);
  XRELOAD(X[31], 3, KE(7)*4096);
  XRELOAD(X[32], 0, KL(0)*4096);
  XRELOAD(X[33], 1, KL(0)*4096);
  XRELOAD(X[34], 2, KL(0)*4096);
  XRELOAD(X[35], 3, KL(0)*4096);
  XRELOAD(X[36], 0, KL(1)*4096);
  XRELOAD(X[37], 1, KL(1)*4096);
  XRELOAD(X[38], 2, KL(1)*4096);
  XRELOAD(X[39], 3, KL(1)*4096);
  XRELOAD(X[40], 0, KL(2)*4096);
  XRELOAD(X[41], 1, KL(2)*4096);
  XRELOAD(X[42], 2, KL(2)*4096);
  XRELOAD(X[43], 3, KL(2)*4096);
    asm volatile("s_waitcnt vmcnt(0)" : "+v"(X[28]),"+v"(X[29]),"+v"(X[30]),"+v"(X[31]));
    if (!__any(max4m(max4m(max4m(max4(X[28]),X[29]),X[30]),X[31]) == SENT)) break; } }
  MGV(Y[0][0], WD[35], X[28]);
  XLOAD(X[47], 3, KL(3)*4096);
  MGV(Y[1][0], WD[35], X[29]);
  MGV(Y[2][0], WD[35], X[30]);
  LRDA(V[15], rds, 15360);
  MGV(Y[3][0], WD[35], X[31]);
  MGV(Y[0][1], WD[36], X[28]);
  MGV(Y[1][1], WD[36], X[29]);
  CKA(15, X[32], 1);
  MGV(Y[2][1], WD[36], X[30]);
  LRDA(V[16], rds, 16384);
  MGV(Y[3][1], WD[36], X[31]);
  CKB(X[32]);
  MGV(Y[0][2], WD[37], X[28]);
  MGV(Y[1][2], WD[37], X[29]);
  CKA(14, X[33], 0);
  MGV(Y[2][2], WD[37], X[30]);
  LRDA(V[17], rds, 17408);
  MGV(Y[3][2], WD[37], X[31]);
  CKB(X[33]);
  MGV(Y[0][3], WD[38], X[28]);
  MGV(Y[1][3], WD[38], X[29]);
  CKA(13, X[34], 0);
  MGV(Y[2][3], WD[38], X[30]);
  LRDA(V[18], rds, 18432);
  MGV(Y[3][3], WD[38], X[31]);
  CKB(X[34]);
  MGV(Y[0][4], WD[39], X[28]);
  MGV(Y[1][4], WD[39], X[29]);
  CKA(12, X[35], 0);
  MGV(Y[2][4], WD[39], X[30]);
  LRDA(V[19], rds, 19456);
  MGV(Y[3][4], WD[39], X[31]);
  CKB(X[35]);
  if (__builtin_expect(__any(mchk == SENT), 0)) { asm volatile("s_waitcnt vmcnt(0)" ::: "memory"); NOUNROLL for (int sp = 0; sp < SPINMAX; sp++) {
  XRELOAD(X[32], 0, KL(0)*4096);
  XRELOAD(X[33], 1, KL(0)*4096);
  XRELOAD(X[34], 2, KL(0)*4096);
  XRELOAD(X[35], 3, KL(0)*4096);
  XRELOAD(X[36], 0, KL(1)*4096);
  XRELOAD(X[37], 1, KL(1)*4096);
  XRELOAD(X[38], 2, KL(1)*4096);
  XRELOAD(X[39], 3, KL(1)*4096);
  XRELOAD(X[40], 0, KL(2)*4096);
  XRELOAD(X[41], 1, KL(2)*4096);
  XRELOAD(X[42], 2, KL(2)*4096);
  XRELOAD(X[43], 3, KL(2)*4096);
  XRELOAD(X[44], 0, KL(3)*4096);
  XRELOAD(X[45], 1, KL(3)*4096);
  XRELOAD(X[46], 2, KL(3)*4096);
  XRELOAD(X[47], 3, KL(3)*4096);
    asm volatile("s_waitcnt vmcnt(0)" : "+v"(X[32]),"+v"(X[33]),"+v"(X[34]),"+v"(X[35]));
    if (!__any(max4m(max4m(max4m(max4(X[32]),X[33]),X[34]),X[35]) == SENT)) break; } }
  asm volatile("s_waitcnt lgkmcnt(0)\n s_barrier" : "+a"(V[0]),"+a"(V[1]),"+a"(V[2]),"+a"(V[3]),"+a"(V[4]),"+a"(V[5]),"+a"(V[6]),"+a"(V[7]),"+a"(V[8]),"+a"(V[9]),"+a"(V[10]),"+a"(V[11]),"+a"(V[12]),"+a"(V[13]),"+a"(V[14]),"+a"(V[15]),"+a"(V[16]),"+a"(V[17]),"+a"(V[18]),"+a"(V[19]) :: "memory");
  MG(Y[2][0], V[0], X[34]);
  CKA(11, X[36], 1);
  MG(Y[3][0], V[0], X[35]);
  CKB(X[36]);
  MG(Y[2][1], V[1], X[34]);
  CKA(10, X[37], 0);
  MG(Y[3][1], V[1], X[35]);
  CKB(X[37]);
  MG(Y[2][2], V[2], X[34]);
  CKA(9, X[38], 0);
  MG(Y[3][2], V[2], X[35]);
  CKB(X[38]);
  MG(Y[2][3], V[3], X[34]);
  CKA(8, X[39], 0);
  MG(Y[3][3], V[3], X[35]);
  CKB(X[39]);
  MG(Y[2][4], V[4], X[34]);
  EDMA(0);
  MG(Y[3][4], V[4], X[35]);
  EDMA(1);
  if (__builtin_expect(__any(mchk == SENT), 0)) { asm volatile("s_waitcnt vmcnt(0)" ::: "memory"); NOUNROLL for (int sp = 0; sp < SPINMAX; sp++) {
  XRELOAD(X[36], 0, KL(1)*4096);
  XRELOAD(X[37], 1, KL(1)*4096);
  XRELOAD(X[38], 2, KL(1)*4096);
  XRELOAD(X[39], 3, KL(1)*4096);
  XRELOAD(X[40], 0, KL(2)*4096);
  XRELOAD(X[41], 1, KL(2)*4096);
  XRELOAD(X[42], 2, KL(2)*4096);
  XRELOAD(X[43], 3, KL(2)*4096);
  XRELOAD(X[44], 0, KL(3)*4096);
  XRELOAD(X[45], 1, KL(3)*4096);
  XRELOAD(X[46], 2, KL(3)*4096);
  XRELOAD(X[47], 3, KL(3)*4096);
    asm volatile("s_waitcnt vmcnt(0)" : "+v"(X[36]),"+v"(X[37]),"+v"(X[38]),"+v"(X[39]));
    if (!__any(max4m(max4m(max4m(max4(X[36]),X[37]),X[38]),X[39]) == SENT)) break; } }
  MG(Y[2][0], V[5], X[38]);
  CKA(9, X[40], 1);
  MG(Y[3][0], V[5], X[39]);
  CKB(X[40]);
  MG(Y[2][1], V[6], X[38]);
  CKA(8, X[41], 0);
  MG(Y[3][1], V[6], X[39]);
  CKB(X[41]);
  MG(Y[2][2], V[7], X[38]);
  CKA(7, X[42], 0);
  MG(Y[3][2], V[7], X[39]);
  CKB(X[42]);
  MG(Y[2][3], V[8], X[38]);
  CKA(6, X[43], 0);
  MG(Y[3][3], V[8], X[39]);
  CKB(X[43]);
  MG(Y[2][4], V[9], X[38]);
  EDMA(2);
  MG(Y[3][4], V[9], X[39]);
  EDMA(3);
  if (__builtin_expect(__any(mchk == SENT), 0)) { asm volatile("s_waitcnt vmcnt(0)" ::: "memory"); NOUNROLL for (int sp = 0; sp < SPINMAX; sp++) {
  XRELOAD(X[40], 0, KL(2)*4096);
  XRELOAD(X[41], 1, KL(2)*4096);
  XRELOAD(X[42], 2, KL(2)*4096);
  XRELOAD(X[43], 3, KL(2)*4096);
  XRELOAD(X[44], 0, KL(3)*4096);
  XRELOAD(X[45], 1, KL(3)*4096);
  XRELOAD(X[46], 2, KL(3)*4096);
  XRELOAD(X[47], 3, KL(3)*4096);
    asm volatile("s_waitcnt vmcnt(0)" : "+v"(X[40]),"+v"(X[41]),"+v"(X[42]),"+v"(X[43]));
    if (!__any(max4m(max4m(max4m(max4(X[40]),X[41]),X[42]),X[43]) == SENT)) break; } }
  MG(Y[2][0], V[10], X[42]);
  CKA(7, X[44], 1);
  MG(Y[3][0], V[10], X[43]);
  CKB(X[44]);
  MG(Y[2][1], V[11], X[42]);
  CKA(6, X[45], 0);
  MG(Y[3][1], V[11], X[43]);
  CKB(X[45]);
  MG(Y[2][2], V[12], X[42]);
  CKA(5, X[46], 0);
  MG(Y[3][2], V[12], X[43]);
  CKB(X[46]);
  MG(Y[2][3], V[13], X[42]);
  CKA(4, X[47], 0);
  MG(Y[3][3], V[13], X[43]);
  CKB(X[47]);
  MG(Y[2][4], V[14], X[42]);
  MG(Y[3][4], V[14], X[43]);
  if (__builtin_expect(__any(mchk == SENT), 0)) { asm volatile("s_waitcnt vmcnt(0)" ::: "memory"); NOUNROLL for (int sp = 0; sp < SPINMAX; sp++) {
  XRELOAD(X[44], 0, KL(3)*4096);
  XRELOAD(X[45], 1, KL(3)*4096);
  XRELOAD(X[46], 2, KL(3)*4096);
  XRELOAD(X[47], 3, KL(3)*4096);
    asm volatile("s_waitcnt vmcnt(0)" : "+v"(X[44]),"+v"(X[45]),"+v"(X[46]),"+v"(X[47]));
    if (!__any(max4m(max4m(max4m(max4(X[44]),X[45]),X[46]),X[47]) == SENT)) break; } }
  MG(Y[2][0], V[15], X[46]);
  MG(Y[3][0], V[15], X[47]);
  MG(Y[2][1], V[16], X[46]);
  MG(Y[3][1], V[16], X[47]);
  MG(Y[2][2], V[17], X[46]);
  MG(Y[3][2], V[17], X[47]);
  MG(Y[2][3], V[18], X[46]);
  MG(Y[3][3], V[18], X[47]);
  MG(Y[2][4], V[19], X[46]);
  MG(Y[3][4], V[19], X[47]);
  MG(Y[1][0], V[0], X[33]);
  MG(Y[1][1], V[1], X[33]);
  YWR(Y[2][0], 5120);
  MG(Y[1][2], V[2], X[33]);
  MG(Y[1][3], V[3], X[33]);
  YWR(Y[3][0], 10240);
  MG(Y[1][4], V[4], X[33]);
  MG(Y[1][0], V[5], X[37]);
  YWR(Y[2][1], 6144);
  MG(Y[1][1], V[6], X[37]);
  MG(Y[1][2], V[7], X[37]);
  YWR(Y[3][1], 11264);
  MG(Y[1][3], V[8], X[37]);
  MG(Y[1][4], V[9], X[37]);
  YWR(Y[2][2], 7168);
  MG(Y[1][0], V[10], X[41]);
  MG(Y[1][1], V[11], X[41]);
  YWR(Y[3][2], 12288);
  MG(Y[1][2], V[12], X[41]);
  MG(Y[1][3], V[13], X[41]);
  YWR(Y[2][3], 8192);
  MG(Y[1][4], V[14], X[41]);
  MG(Y[1][0], V[15], X[45]);
  YWR(Y[3][3], 13312);
  MG(Y[1][1], V[16], X[45]);
  MG(Y[1][2], V[17], X[45]);
  YWR(Y[2][4], 9216);
  MG(Y[1][3], V[18], X[45]);
  MG(Y[1][4], V[19], X[45]);
  YWR(Y[3][4], 14336);
  MG(Y[0][0], V[0], X[32]);
  asm volatile("s_waitcnt vmcnt(0)" ::: "memory");
  MG(Y[0][1], V[1], X[32]);
  MG(Y[0][2], V[2], X[32]);
  LRD(EX[0], exa, 0);
  MG(Y[0][3], V[3], X[32]);
  MG(Y[0][4], V[4], X[32]);
  LRD(EX[1], exa, 16);
  MG(Y[0][0], V[5], X[36]);
  YWR(Y[1][0], 0);
  MG(Y[0][1], V[6], X[36]);
  LRD(EX[2], exa, 32);
  MG(Y[0][2], V[7], X[36]);
  YWR(Y[1][1], 1024);
  MG(Y[0][3], V[8], X[36]);
  LRD(EX[3], exa, 48);
  MG(Y[0][4], V[9], X[36]);
  YWR(Y[1][2], 2048);
  MG(Y[0][0], V[10], X[40]);
  LRD(EX[4], exa, 64);
  MG(Y[0][1], V[11], X[40]);
  YWR(Y[1][3], 3072);
  MG(Y[0][2], V[12], X[40]);
  LRD(EG[0], ega, 0);
  MG(Y[0][3], V[13], X[40]);
  YWR(Y[1][4], 4096);
  MG(Y[0][4], V[14], X[40]);
  LRD(EG[1], ega, 16);
  MG(Y[0][0], V[15], X[44]);
  MG(Y[0][1], V[16], X[44]);
  LRD(EG[2], ega, 32);
  MG(Y[0][2], V[17], X[44]);
  MG(Y[0][3], V[18], X[44]);
  LRD(EG[3], ega, 48);
  MG(Y[0][4], V[19], X[44]);
  LRD(EG[4], ega, 64);
    asm volatile("s_waitcnt lgkmcnt(0)\n s_barrier\n s_nop 5" : "+a"(Y[0][0]), "+a"(Y[0][1]), "+a"(Y[0][2]), "+a"(Y[0][3]), "+a"(Y[0][4]),
                 "+v"(EX[0]), "+v"(EX[1]), "+v"(EX[2]), "+v"(EX[3]), "+v"(EX[4]), "+v"(EG[0]), "+v"(EG[1]), "+v"(EG[2]), "+v"(EG[3]), "+v"(EG[4]) :: "memory");
    f32x4 mine[5];
    #pragma unroll
    for (int j = 0; j < 5; j++) mine[j] = Y[0][j];
    #pragma unroll
    for (u32 o = 1; o < 4; o++) {
      const u32 sw = (w + o) & 3, d = 3 - o;
      const u32 ad = 81920 + sw*20480 + d*5120 + l16;
      #pragma unroll
      for (int j = 0; j < 5; j++) mine[j] += *(const f32x4*)(smd + ad + j*1024);
    }
    const u32 aa = rho >> 3, mrow = 32*(4*(aa >> 5) + ((rho >> 1) & 3)) + (aa & 31);
    const u32 sh = (rho & 1) ? 0 : 16;
    u32 o[10];
    #pragma unroll
    for (int j = 0; j < 5; j++) {
      float r0 = __uint_as_float((EX[j].x << sh) & 0xffff0000u) + bflo(EG[j].x)*mine[j].x, r1 = __uint_as_float((EX[j].y << sh) & 0xffff0000u) + bfhi(EG[j].x)*mine[j].y;
      float r2 = __uint_as_float((EX[j].z << sh) & 0xffff0000u) + bflo(EG[j].z)*mine[j].z, r3 = __uint_as_float((EX[j].w << sh) & 0xffff0000u) + bfhi(EG[j].z)*mine[j].w;
      o[2*j] = pkbf(r0, r1); o[2*j + 1] = pkbf(r2, r3);
    }
    u16* op = out + (size_t)(2*mrow + (rho & 1))*640 + n0;
    *(u32x4a8*)op = u32x4{o[0], o[1], o[2], o[3]};
    *(u32x4a8*)(op + 8) = u32x4{o[4], o[5], o[6], o[7]};
    *(u32x2*)(op + 16) = u32x2{o[8], o[9]};
  }
  asm volatile("fused_code_end:" ::: "memory");
}

extern "C" void kernel_launch(void* const* d_in, const int* in_sizes, int n_in, void* d_out, int out_size, void* d_ws, size_t ws_size, hipStream_t stream){
  (void)hipFuncSetAttribute((const void*)fused, hipFuncAttributeMaxDynamicSharedMemorySize, 163840);
  fused<<<256, 256, 163840, stream>>>((const u16*)d_in[0], (const u16*)d_in[1], (const u16*)d_in[2], (const u16*)d_in[4], (const u16*)d_in[5], (const u16*)d_in[6],
                                      (const u16*)d_in[7], (const u16*)d_in[8], (char*)d_ws, (u16*)d_out);
}
```
